```python
import torch

def _softmax(x, axis=-1):
    return torch.softmax(x, dim=axis)
def _gelu(x):
    return torch.nn.functional.gelu(x, approximate="tanh")
def _silu(x):
    return torch.nn.functional.silu(x)
def _sigmoid(x):
    return torch.sigmoid(x)
def _layernorm(x, axis=-1, eps=1e-5):
    m = x.mean(dim=axis, keepdim=True)
    v = x.var(dim=axis, keepdim=True, unbiased=False)
    return (x - m) / torch.sqrt(v + eps)
def _scatter_add(tgt, idx, src, axis=0):
    return tgt.clone().index_add_(axis, idx, src)
def _segment_sum(src, seg, n):
    out = torch.zeros((n,) + src.shape[1:], dtype=src.dtype)
    return out.index_add_(0, seg, src)
def _pad(x, pw):
    flat = tuple(v for pair in reversed(pw) for v in pair)
    return torch.nn.functional.pad(x, flat)

kernel_name = 'parallel_sum__mha_block__dense_activation_gelu__prompt'

def setup_inputs():

    _w = (lambda a: (lambda t: t.to(torch.bfloat16) if t.is_floating_point() else t)(torch.as_tensor(a)))
    def _A_setup():
        B, S, D, H = 1, 2048, 640, 32
        return {
            "x": _w((lambda *s: torch.randn(*s, dtype=torch.bfloat16) * 0.05)(B, S, D)),
            "wqkv": _w((lambda *s: torch.randn(*s, dtype=torch.bfloat16) * 0.05)(D, 3 * D)),
            "wo": _w((lambda *s: torch.randn(*s, dtype=torch.bfloat16) * 0.05)(D, D)),
        }
    _A_in = _A_setup()

    def _B_setup():
        B, S, D, F = 1, 2048, 640, 1728
        return {
            "x": _w((lambda *s: torch.randn(*s, dtype=torch.bfloat16) * 0.05)(B, S, D)),
            "w1": _w((lambda *s: torch.randn(*s, dtype=torch.bfloat16) * 1.6)(D, F) * (D ** -0.5)),
            "b1": _w((lambda *s: torch.randn(*s, dtype=torch.bfloat16) * 1.6)(F) * 0.01),
            "w2": _w((lambda *s: torch.randn(*s, dtype=torch.bfloat16) * 0.2)(F, D) * (F ** -0.5)),
            "b2": _w((lambda *s: torch.randn(*s, dtype=torch.bfloat16) * 0.2)(D) * 0.01),
        }
    _B_in = _B_setup()

    return {
        "A_x": _A_in["x"],
        "A_wqkv": _A_in["wqkv"],
        "A_wo": _A_in["wo"],
        "B_x": _B_in["x"],
        "B_w1": _B_in["w1"],
        "B_b1": _B_in["b1"],
        "B_w2": _B_in["w2"],
        "B_b2": _B_in["b2"],
    }

def reference(A_x, A_wqkv, A_wo, B_x, B_w1, B_b1, B_w2, B_b2):
    def _A_ref(x, wqkv, wo):
        B, S, D = x.shape
        H = 32
        Dh = D // H
        qkv = (lambda x, *d: x.permute(*d))((x @ wqkv).reshape(B, S, 3, H, Dh), 2, 0, 3, 1, 4)
        q, k, v = qkv[0], qkv[1], qkv[2]
        scores = torch.einsum("bhqd,bhkd->bhqk", q, k) * (Dh ** -0.5)
        p = _softmax(scores, axis=-1)
        o = torch.einsum("bhqk,bhkd->bhqd", p, v)
        return (lambda x, *d: x.permute(*d))(o, 0, 2, 1, 3).reshape(B, S, D) @ wo

    def _B_ref(x, w1, b1, w2, b2):
        h = torch.einsum("bsd,df->bsf", x, w1) + b1
        h = _gelu(h)
        return torch.einsum("bsf,fd->bsd", h, w2) + b2

    x = A_x
    xn = _layernorm(x, axis=-1)
    return x + _A_ref(xn, A_wqkv, A_wo) + _B_ref(xn, B_w1, B_b1, B_w2, B_b2)
```

```cpp
#include <hip/hip_runtime.h>
#include <hip/hip_bf16.h>
#include <stdint.h>

using bf16x8 = __attribute__((ext_vector_type(8))) short;
using s16x4 = __attribute__((ext_vector_type(4))) short;
using f32x16 = __attribute__((ext_vector_type(16))) float;
using f32x4 = __attribute__((ext_vector_type(4))) float;
using f32x2 = __attribute__((ext_vector_type(2))) float;
using u32x4 = __attribute__((ext_vector_type(4))) unsigned;
using u32x2 = __attribute__((ext_vector_type(2))) unsigned;
typedef __attribute__((address_space(3))) char* lds_ptr;

#define DEVI __device__ __forceinline__
#define SBAR() __builtin_amdgcn_sched_barrier(0)
#define PIN(x) asm volatile("" : "+v"(x))
#define MFMA16(a, b, c) __builtin_amdgcn_mfma_f32_32x32x16_bf16(a, b, c, 0, 0, 0)
#define MFMA8(a, b, c) __builtin_amdgcn_mfma_f32_32x32x8bf16_1k(a, b, c, 0, 0, 0)
#define MFMAS(a, b, c) __builtin_amdgcn_mfma_f32_16x16x32_bf16(a, b, c, 0, 0, 0)
#define WAIT_BAR(N) asm volatile("s_waitcnt vmcnt(" #N ") lgkmcnt(0)\n\ts_barrier" ::: "memory")
#define LGKM_BAR() asm volatile("s_waitcnt lgkmcnt(0)\n\ts_barrier" ::: "memory")
#define RAW_BAR() asm volatile("s_barrier" ::: "memory")

constexpr int SEQ = 2048, DM = 640, FF = 1728;
constexpr int QT = 1280, KVT = 2624;
constexpr size_t WS_QI = 0;
constexpr size_t WS_KVI = WS_QI + 32 * 64 * QT;
constexpr size_t WS_HI = WS_KVI + 32 * 64 * KVT + 4096;
constexpr size_t WS_W2I = WS_HI + 64 * 108 * 1024;
constexpr size_t WS_WOI = WS_W2I + 20 * 108 * 1024;
constexpr size_t WS_OI = WS_WOI + 20 * 40 * 1024;
constexpr size_t WS_PART = WS_OI + 64 * 40 * 1024;
constexpr float LOG2E = 1.4426950408889634f;
constexpr float QSCALE = 0.22360679774997896f * LOG2E;
constexpr float GC1 = -2.f * 0.7978845608028654f * LOG2E, GC2 = GC1 * 0.044715f;

typedef __bf16 bf16x2_t __attribute__((ext_vector_type(2)));
DEVI unsigned cvtpk(float lo, float hi) { return __builtin_bit_cast(unsigned, __builtin_convertvector(f32x2{lo, hi}, bf16x2_t)); }
DEVI void glds16(const void* g, unsigned lds_base) {
  unsigned sv;
  asm volatile("s_mov_b32 %0, m0\n\ts_mov_b32 m0, %2\n\ts_nop 0\n\tglobal_load_lds_dwordx4 %1, off\n\ts_mov_b32 m0, %0" : "=&s"(sv) : "v"(g), "s"(lds_base) : "memory");
}
DEVI s16x4 ldtr(lds_ptr p) { return __builtin_amdgcn_ds_read_tr16_b64_v4i16((__attribute__((address_space(3))) s16x4*)p); }
DEVI bf16x8 ldtr2(lds_ptr p, int off2) { s16x4 a = ldtr(p), b = ldtr(p + off2); return __builtin_shufflevector(a, b, 0, 1, 2, 3, 4, 5, 6, 7); }
template <typename T> DEVI T ldl(lds_ptr p) { return *(__attribute__((address_space(3))) T*)p; }
template <typename T> DEVI void stl(lds_ptr p, T v) { *(__attribute__((address_space(3))) T*)p = v; }
DEVI float blo(unsigned d) { return __uint_as_float(d << 16); }
DEVI float bhi(unsigned d) { return __uint_as_float(d & 0xffff0000u); }
DEVI u32x4 pack8(const float* v) { return u32x4{cvtpk(v[0], v[1]), cvtpk(v[2], v[3]), cvtpk(v[4], v[5]), cvtpk(v[6], v[7])}; }
DEVI float gelu(float u) {
  float z = u * __builtin_fmaf(GC2, u * u, GC1);
  return u * __builtin_amdgcn_rcpf(1.f + __builtin_amdgcn_exp2f(z));
}

constexpr int K1_STG = 49152, K1_SPARE = 3 * K1_STG, K1_LDS = K1_SPARE + 16384;

struct K1Frag { bf16x8 x[2], w[2]; };

template <bool VFIRST>
DEVI void k1_main(f32x16 (&acc)[2][2], f32x4 (&cs)[2], float (&sum)[2], float (&sq)[2], lds_ptr lds, const char* xsrc, const char* wsrc,
                  long wstep, unsigned ldsx, unsigned ldsw, int xa0, int wa0, int wa1, bf16x8 sel) {
  lds_ptr xp[4], wp0[4], wp1[4];
#pragma unroll
  for (int p = 0; p < 4; ++p) { xp[p] = lds + (xa0 ^ (p * 32)); wp0[p] = lds + 16384 + (wa0 ^ (p * 8192)); wp1[p] = lds + 16384 + (wa1 ^ (p * 8192)); }
  K1Frag A, B;
  B.x[0] = B.x[1] = B.w[0] = B.w[1] = bf16x8{};
#define K1_LD(F, p) do { F.x[0] = ldl<bf16x8>(xp[p]); F.w[0] = ldtr2(wp0[p], 2048); F.x[1] = ldl<bf16x8>(xp[p] + 4096); F.w[1] = ldtr2(wp1[p], 2048); } while (0)
#define K1_MM(F) do { \
    if (VFIRST) { acc[0][0] = MFMA16(F.x[0], F.w[0], acc[0][0]); acc[0][1] = MFMA16(F.x[1], F.w[0], acc[0][1]); } \
    else { acc[0][0] = MFMA16(F.w[0], F.x[0], acc[0][0]); acc[0][1] = MFMA16(F.w[0], F.x[1], acc[0][1]); } \
    acc[1][0] = MFMA16(F.w[1], F.x[0], acc[1][0]); acc[1][1] = MFMA16(F.w[1], F.x[1], acc[1][1]); } while (0)
#define K1_CS(F) do { cs[0] = MFMAS(F.w[0], sel, cs[0]); cs[1] = MFMAS(F.w[1], sel, cs[1]); } while (0)
  int nb = 2 * K1_STG;
  for (int s = 0; s < 10; ++s) {
    if (s < 9) WAIT_BAR(6); else WAIT_BAR(0);
    if (s < 8) {
#pragma unroll
      for (int j = 0; j < 4; ++j) { glds16(wsrc, ldsw + nb + j * 8192); wsrc += wstep; }
      glds16(xsrc, ldsx + nb); glds16(xsrc + 64 * 1280, ldsx + nb + 8192); xsrc += 128;
      nb = (nb == 2 * K1_STG) ? 0 : nb + K1_STG;
    }
    K1_LD(A, 0); SBAR();
    K1_MM(B); SBAR();
    K1_LD(B, 1); SBAR();
    K1_MM(A); K1_CS(A);
#pragma unroll
    for (int t = 0; t < 2; ++t) {
      u32x4 d = __builtin_bit_cast(u32x4, A.x[t]);
#pragma unroll
      for (int e = 0; e < 4; ++e) { float lo = blo(d[e]), hi = bhi(d[e]); sum[t] += lo; sq[t] = __builtin_fmaf(lo, lo, sq[t]); sum[t] += hi; sq[t] = __builtin_fmaf(hi, hi, sq[t]); }
    }
    SBAR();
    K1_LD(A, 2); SBAR();
    K1_MM(B); SBAR();
    K1_LD(B, 3); SBAR();
    K1_MM(A); K1_CS(A); SBAR();
    const int dl = (s % 3 == 2) ? -2 * K1_STG : K1_STG;
#pragma unroll
    for (int p = 0; p < 4; ++p) { xp[p] += dl; wp0[p] += dl; wp1[p] += dl; }
  }
  K1_MM(B);
}

__global__ __launch_bounds__(512, 2) void k1(const char* __restrict__ x, const char* __restrict__ wqkv, const char* __restrict__ w1,
                                             const char* __restrict__ b1, const char* __restrict__ w2, const char* __restrict__ wo, char* __restrict__ ws) {
  extern __shared__ __attribute__((aligned(16))) char smem[];
  const lds_ptr lds = (lds_ptr)smem;
  const unsigned lds0 = (unsigned)(uintptr_t)smem;
  const int lane = threadIdx.x & 63, w = __builtin_amdgcn_readfirstlane(threadIdx.x >> 6), r = lane & 31, hh = lane >> 5;
  const int bid = blockIdx.x, g = 2 * (bid & 7) + ((bid >> 3) & 1), mg = bid >> 4, mh = w & 1, np = w >> 1;

  const int gw = bid * 8 + w;
  char* rdst[2];
#pragma unroll
  for (int i = 0; i < 2; ++i) {
    const int b = gw + 2048 * i, slot = lane >> 2;
    if (b < 2160) {
      const int nu = b % 20, ks = b / 20;
      glds16(w2 + (16 * ks + slot) * 1280 + nu * 64 + (lane & 3) * 16, lds0 + K1_SPARE + w * 2048 + i * 1024);
      rdst[i] = ws + WS_W2I + (nu * 108 + ks) * 1024 + lane * 16;
    } else if (b < 2960) {
      const int nu = (b - 2160) % 20, ks = (b - 2160) / 20;
      const int row = ks < 32 ? 20 * ks + slot : 20 * (4 * (ks - 32) + 2 * ((slot >> 2) & 1) + (slot >> 3)) + 16 + (slot & 3);
      glds16(wo + row * 1280 + nu * 64 + (lane & 3) * 16, lds0 + K1_SPARE + w * 2048 + i * 1024);
      rdst[i] = ws + WS_WOI + (nu * 40 + ks) * 1024 + lane * 16;
    } else rdst[i] = nullptr;
  }

  const int u1 = np == 3 ? 2 : np + 3;
  const int fb = 112 * g + 32 * u1 - 80;
  const bool mlp_ok = fb < FF;
  u32x2 bias[4];
#pragma unroll
  for (int i = 0; i < 4; ++i) { int f = fb + 8 * i + 4 * hh; f = f < 0 ? 0 : (f > FF - 4 ? FF - 4 : f); bias[i] = *(const u32x2*)(b1 + f * 2); }

  const char* wsrc; long wstep;
  {
    const int c = (lane & 31) ^ ((2 * (w & 1) + hh) << 2), k0 = 2 * w + hh;
    if (c >= 10 && c < 24) {
      int f = 112 * g + 8 * (c - 10); if (f >= FF) f = 112 * g;
      wsrc = w1 + k0 * 3456 + f * 2; wstep = 16 * 3456;
    } else {
      int col;
      if (c < 8) col = (c >> 2) * 640 + 8 * ((c & 3) + ((c >> 1) & 1));
      else if (c < 10) col = (c - 8) * 640 + 16;
      else col = 1280 + 8 * (c < 28 ? c - 24 : 4);
      wsrc = wqkv + k0 * 3840 + (40 * g + col) * 2; wstep = 16 * 3840;
    }
  }
  const char* xsrc = x + (128 * mg + 8 * w + (lane >> 3)) * 1280 + (((lane & 7) ^ ((4 * w + (lane >> 4)) & 7)) * 16);
  const unsigned ldsx = lds0 + w * 1024, ldsw = lds0 + 16384 + w * 1024;
#pragma unroll
  for (int s = 0; s < 2; ++s) {
#pragma unroll
    for (int j = 0; j < 4; ++j) { glds16(wsrc, ldsw + s * K1_STG + j * 8192); wsrc += wstep; }
    glds16(xsrc, ldsx + s * K1_STG); glds16(xsrc + 64 * 1280, ldsx + s * K1_STG + 8192); xsrc += 128;
  }

  const int perm = np ^ mh;
  const int xa0 = (64 * mh + r) * 128 + (((2 * perm + hh) ^ ((r >> 1) & 7)) * 16);
  const int q4 = (lane & 15) >> 2, cb = 32 * ((lane >> 4) & 1) + 8 * (lane & 3);
  const int ub0 = np < 2 ? 64 * np : (np == 2 ? 384 : 424);
  const int wrow = (16 * perm + 8 * hh + q4) * 512;
  const int wa0 = wrow + ((ub0 + cb) ^ (q4 << 6)), wa1 = wrow + ((64 * u1 + cb) ^ (q4 << 6));
  const short one = ((lane & 15) == ((lane >> 4) & 1)) ? (short)0x3f80 : (short)0;
  const bf16x8 sel = {one, one, one, one, one, one, one, one};

  f32x16 acc[2][2] = {};
  f32x4 cs[2] = {};
  float sum[2] = {}, sq[2] = {};
  asm volatile("s_waitcnt vmcnt(12)" ::: "memory");
  const lds_ptr rp = lds + K1_SPARE + w * 2048 + (4 * hh + q4) * 64 + cb;
  bf16x8 rl0 = ldtr2(rp, 512), rl1 = ldtr2(rp + 1024, 512);

  if (np < 2) k1_main<false>(acc, cs, sum, sq, lds, xsrc, wsrc, wstep, ldsx, ldsw, xa0, wa0, wa1, sel);
  else k1_main<true>(acc, cs, sum, sq, lds, xsrc, wsrc, wstep, ldsx, ldsw, xa0, wa0, wa1, sel);

  *(bf16x8*)rdst[0] = rl0;
  if (gw + 2048 < 2960) *(bf16x8*)rdst[1] = rl1;

  const lds_ptr ST = lds + K1_SPARE, CS = lds + K1_SPARE + 4096, PV = lds + K1_SPARE + 8192 + w * 512;
  {
    auto a = __builtin_amdgcn_permlane32_swap(__float_as_uint(sum[0]), __float_as_uint(sum[1]), false, false);
    auto b = __builtin_amdgcn_permlane32_swap(__float_as_uint(sq[0]), __float_as_uint(sq[1]), false, false);
    stl<f32x2>(ST + (np * 128 + 64 * mh + lane) * 8, f32x2{__uint_as_float(a[0]) + __uint_as_float(a[1]), __uint_as_float(b[0]) + __uint_as_float(b[1])});
    if ((lane & 15) < 2) {
      stl<f32x4>(CS + ((np * 2 + 0) * 2 + mh) * 128 + (16 * (lane & 1) + 4 * (lane >> 4)) * 4, cs[0]);
      stl<f32x4>(CS + ((np * 2 + 1) * 2 + mh) * 128 + (16 * (lane & 1) + 4 * (lane >> 4)) * 4, cs[1]);
    }
  }
  LGKM_BAR();
  float rs[2], nm[2];
  {
    f32x2 t = ldl<f32x2>(ST + (64 * mh + lane) * 8);
#pragma unroll
    for (int i = 1; i < 4; ++i) t += ldl<f32x2>(ST + (i * 128 + 64 * mh + lane) * 8);
    const float mean = t[0] * (1.f / 640.f);
    const float var = __builtin_fmaxf(__builtin_fmaf(-mean, mean, t[1] * (1.f / 640.f)), 0.f);
    const float rstd = __builtin_amdgcn_rsqf(var + 1e-5f), nmr = -mean * rstd;
    if (np >= 2) { stl<float>(PV + lane * 4, rstd); stl<float>(PV + 256 + lane * 4, nmr); }
    auto a = __builtin_amdgcn_permlane32_swap(__float_as_uint(rstd), __float_as_uint(rstd), false, false);
    auto b = __builtin_amdgcn_permlane32_swap(__float_as_uint(nmr), __float_as_uint(nmr), false, false);
    rs[0] = __uint_as_float(a[0]); rs[1] = __uint_as_float(a[1]); nm[0] = __uint_as_float(b[0]); nm[1] = __uint_as_float(b[1]);
  }
  const int mt0 = 4 * mg + 2 * mh;
  if (np < 2) {
    f32x4 c4[4];
#pragma unroll
    for (int i = 0; i < 4; ++i) c4[i] = ldl<f32x4>(CS + (np * 4 + 0) * 128 + (8 * i + 4 * hh) * 4) + ldl<f32x4>(CS + (np * 4 + 1) * 128 + (8 * i + 4 * hh) * 4);
    const float sc = np == 0 ? QSCALE : 1.f;
    const int tb = np == 0 ? QT : KVT;
    char* dst = ws + (np == 0 ? WS_QI : WS_KVI) + (size_t)(2 * g * 64 + mt0) * tb + lane * 16;
#pragma unroll
    for (int t = 0; t < 2; ++t) {
      const float a = rs[t] * sc, b = nm[t] * sc;
      float v[16];
#pragma unroll
      for (int i = 0; i < 16; ++i) v[i] = __builtin_fmaf(a, acc[0][t][i], b * c4[i >> 2][i & 3]);
      *(u32x4*)(dst + t * tb) = pack8(v);
      *(u32x4*)(dst + (64 + t) * tb) = pack8(v + 8);
    }
  } else {
    const float c = ldl<float>(CS + (np * 4 + 0) * 128 + r * 4) + ldl<float>(CS + (np * 4 + 1) * 128 + r * 4);
    char* dst = ws + WS_KVI + (size_t)((2 * g + np - 2) * 64 + mt0) * KVT + 1280 + hh * 336 + r * 16;
#pragma unroll
    for (int t = 0; t < 2; ++t) {
      float v[16];
#pragma unroll
      for (int i = 0; i < 4; ++i) {
        const f32x4 a = ldl<f32x4>(PV + (32 * t + 8 * i + 4 * hh) * 4), b = ldl<f32x4>(PV + 256 + (32 * t + 8 * i + 4 * hh) * 4);
#pragma unroll
        for (int e = 0; e < 4; ++e) v[4 * i + e] = r == 20 ? 1.f : __builtin_fmaf(a[e], acc[0][t][4 * i + e], b[e] * c);
      }
      if (r <= 20) { *(u32x4*)(dst + t * KVT) = pack8(v); *(u32x4*)(dst + t * KVT + 672) = pack8(v + 8); }
    }
  }
  if (mlp_ok) {
    f32x4 c4[4];
#pragma unroll
    for (int i = 0; i < 4; ++i) c4[i] = ldl<f32x4>(CS + (np * 4 + 2) * 128 + (8 * i + 4 * hh) * 4) + ldl<f32x4>(CS + (np * 4 + 3) * 128 + (8 * i + 4 * hh) * 4);
    char* hdst = ws + WS_HI + (size_t)(mt0 * 108 + 7 * g + 2 * u1 - 5) * 1024 + lane * 16;
#pragma unroll
    for (int t = 0; t < 2; ++t) {
      float v[16];
      if (np == 3) {
#pragma unroll
        for (int i = 0; i < 8; ++i) v[i] = __builtin_fmaf(rs[t], acc[1][t][i], nm[t] * c4[i >> 2][i & 3]);
        *(u32x2*)(ws + WS_QI + (size_t)((2 * g + hh) * 64 + mt0 + t) * QT + 1024 + r * 8) = u32x2{cvtpk(v[0] * QSCALE, v[1] * QSCALE), cvtpk(v[2] * QSCALE, v[3] * QSCALE)};
        *(u32x2*)(ws + WS_KVI + (size_t)((2 * g + hh) * 64 + mt0 + t) * KVT + 1024 + r * 8) = u32x2{cvtpk(v[4], v[5]), cvtpk(v[6], v[7])};
      } else {
#pragma unroll
        for (int i = 0; i < 8; ++i) {
          const unsigned bd = bias[i >> 2][(i >> 1) & 1];
          v[i] = gelu(__builtin_fmaf(rs[t], acc[1][t][i], __builtin_fmaf(nm[t], c4[i >> 2][i & 3], (i & 1) ? bhi(bd) : blo(bd))));
        }
        *(u32x4*)(hdst + t * 108 * 1024) = pack8(v);
      }
#pragma unroll
      for (int i = 8; i < 16; ++i) {
        const unsigned bd = bias[i >> 2][(i >> 1) & 1];
        v[i] = gelu(__builtin_fmaf(rs[t], acc[1][t][i], __builtin_fmaf(nm[t], c4[i >> 2][i & 3], (i & 1) ? bhi(bd) : blo(bd))));
      }
      *(u32x4*)(hdst + t * 108 * 1024 + 1024) = pack8(v + 8);
    }
  }
}

constexpr int K2_CH = 16 * KVT, K2_RED = 2 * K2_CH, K2_LDS = K2_RED + 61440;

DEVI float max16(const f32x16& s) {
  float a = __builtin_fmaxf(__builtin_fmaxf(s[0], s[1]), s[2]), b = __builtin_fmaxf(__builtin_fmaxf(s[3], s[4]), s[5]);
  a = __builtin_fmaxf(__builtin_fmaxf(a, s[6]), s[7]); b = __builtin_fmaxf(__builtin_fmaxf(b, s[8]), s[9]);
  a = __builtin_fmaxf(__builtin_fmaxf(a, s[10]), s[11]); b = __builtin_fmaxf(__builtin_fmaxf(b, s[12]), s[13]);
  a = __builtin_fmaxf(__builtin_fmaxf(a, s[14]), s[15]); a = __builtin_fmaxf(a, b);
  auto t = __builtin_amdgcn_permlane32_swap(__float_as_uint(a), __float_as_uint(a), false, false);
  return __builtin_fmaxf(__uint_as_float(t[0]), __uint_as_float(t[1]));
}

__global__ __launch_bounds__(768, 3) void k2(const char* __restrict__ x, const char* __restrict__ b2, char* __restrict__ ws) {
  extern __shared__ __attribute__((aligned(16))) char smem[];
  const lds_ptr lds = (lds_ptr)smem;
  const unsigned lds0 = (unsigned)(uintptr_t)smem;
  const int lane = threadIdx.x & 63, w = __builtin_amdgcn_readfirstlane(threadIdx.x >> 6), r = lane & 31, hh = lane >> 5;
  const int bid = blockIdx.x;
  if (w < 8) {
    const int h = 4 * (bid & 7) + ((bid >> 3) & 3), mt = 8 * (bid >> 5) + w;
    const char* kv = ws + WS_KVI + (size_t)h * 64 * KVT;
    const char* src = kv + w * 1024 + lane * 16;
#define K2_FILL(c) do { _Pragma("unroll") for (int j = 0; j < 5; ++j) glds16(src + (c) * K2_CH + j * 8192, lds0 + ((c) & 1) * K2_CH + w * 1024 + j * 8192); \
      if (w == 0) glds16(src + (c) * K2_CH + 40960, lds0 + ((c) & 1) * K2_CH + 40960); } while (0)
    const char* qp = ws + WS_QI + (size_t)(h * 64 + mt) * QT;
    const bf16x8 qm = *(const bf16x8*)(qp + lane * 16);
    s16x4 qr = *(const s16x4*)(qp + 1024 + r * 8);
    const int kro = hh ? 1600 : 1024 + r * 8, vo = 1280 + hh * 336 + (r < 20 ? r : 20) * 16;
    const bf16x8 km0 = *(const bf16x8*)(kv + lane * 16);
    const s16x4 kr0 = *(const s16x4*)(kv + kro);
    K2_FILL(0); K2_FILL(1);
    if (hh) qr = s16x4{};
    const s16x4 qr0 = qr;
    f32x16 sA = MFMA8(kr0, qr, f32x16{}); sA = MFMA16(km0, qm, sA);
    {
      const float m = max16(sA);
      const unsigned nb = cvtpk(-m, 0.f);
      if (hh) qr[0] = (short)nb;
      const float mb = blo(nb);
#pragma unroll
      for (int i = 0; i < 16; ++i) sA[i] += mb;
    }
    f32x16 o = {}, sB;
    if (w == 0) WAIT_BAR(6); else WAIT_BAR(5);
    lds_ptr kmp = lds + lane * 16, krp = lds + kro, vp = lds + vo;
    bf16x8 v0 = ldl<bf16x8>(vp), v1 = ldl<bf16x8>(vp + 672), km; s16x4 kr;
#define K2_EXP4(S, i, P, j) do { P[j] = cvtpk(__builtin_amdgcn_exp2f(S[i]), __builtin_amdgcn_exp2f(S[i + 1])); P[j + 1] = cvtpk(__builtin_amdgcn_exp2f(S[i + 2]), __builtin_amdgcn_exp2f(S[i + 3])); } while (0)
#define K2_TILE(S, N, off) do { u32x4 p0, p1; \
      kr = ldl<s16x4>(krp + (off)); km = ldl<bf16x8>(kmp + (off)); SBAR(); \
      K2_EXP4(S, 0, p0, 0); K2_EXP4(S, 4, p0, 2); SBAR(); \
      o = MFMA16(v0, __builtin_bit_cast(bf16x8, p0), o); N = MFMA8(kr, qr, f32x16{}); SBAR(); \
      K2_EXP4(S, 8, p1, 0); SBAR(); \
      N = MFMA16(km, qm, N); SBAR(); \
      K2_EXP4(S, 12, p1, 2); SBAR(); \
      o = MFMA16(v1, __builtin_bit_cast(bf16x8, p1), o); SBAR(); \
      v0 = ldl<bf16x8>(vp + (off)); v1 = ldl<bf16x8>(vp + (off) + 672); SBAR(); } while (0)
    for (int c = 0; c < 4; ++c) {
#pragma unroll
      for (int t = 0; t < 14; t += 2) { K2_TILE(sA, sB, (t + 1) * KVT); K2_TILE(sB, sA, (t + 2) * KVT); }
      K2_TILE(sA, sB, 15 * KVT);
      int dl = 0;
      if (c < 3) {
        WAIT_BAR(0);
        if (c < 2) K2_FILL(c + 2);
        dl = (c & 1) ? -K2_CH : K2_CH;
      }
      kmp += dl; krp += dl; vp += dl;
      K2_TILE(sB, sA, 0);
    }
    auto lt = __builtin_amdgcn_permlane32_swap(__float_as_uint(o[8]), __float_as_uint(o[8]), false, false);
    float l = __uint_as_float(lt[1]);
    float om = 0.f;
#pragma unroll
    for (int i = 0; i < 12; ++i) om = __builtin_fmaxf(om, __builtin_fabsf(o[i]));
    const bool bad = !(l >= 0x1p-30f && l <= 0x1p60f && om <= 0x1p126f);
    if (__builtin_amdgcn_ballot_w64(bad) != 0) {
      float m = -__builtin_inff();
      o = f32x16{};
      for (int t = 0; t < 64; ++t) {
        const char* tp = kv + t * KVT;
        const bf16x8 a = *(const bf16x8*)(tp + lane * 16), va = *(const bf16x8*)(tp + vo), vb = *(const bf16x8*)(tp + vo + 672);
        const s16x4 b = *(const s16x4*)(tp + kro);
        f32x16 s = MFMA8(b, qr0, f32x16{}); s = MFMA16(a, qm, s);
        const float mn = __builtin_fmaxf(m, max16(s)), al = __builtin_amdgcn_exp2f(m - mn);
        m = mn;
        float p[16];
#pragma unroll
        for (int i = 0; i < 16; ++i) { p[i] = __builtin_amdgcn_exp2f(s[i] - mn); o[i] *= al; }
        o = MFMA16(va, __builtin_bit_cast(bf16x8, pack8(p)), o);
        o = MFMA16(vb, __builtin_bit_cast(bf16x8, pack8(p + 8)), o);
      }
      lt = __builtin_amdgcn_permlane32_swap(__float_as_uint(o[8]), __float_as_uint(o[8]), false, false);
      l = __uint_as_float(lt[1]);
    }
    const float il = __builtin_amdgcn_rcpf(l);
    float v[12];
#pragma unroll
    for (int i = 0; i < 12; ++i) v[i] = o[i] * il;
    char* od = ws + WS_OI + (size_t)mt * 40 * 1024;
    *(u32x4*)(od + h * 1024 + lane * 16) = pack8(v);
    if (!hh) *(u32x2*)(od + (32 + (h >> 2)) * 1024 + ((h >> 1) & 1) * 512 + r * 16 + (h & 1) * 8) = u32x2{cvtpk(v[8], v[9]), cvtpk(v[10], v[11])};
  } else {
    __builtin_amdgcn_s_setprio(2);
    const int i4 = w - 8, xcd = bid & 7, j = bid >> 3;
    const int mt = 16 * (xcd >> 1) + (j >> 1), nb = 2 * (xcd & 1) + (j & 1);
    const char* hp = ws + WS_HI + (size_t)(mt * 108 + 27 * i4) * 1024 + lane * 16;
    const char* wp = ws + WS_W2I + (size_t)(nb * 5 * 108 + 27 * i4) * 1024 + lane * 16;
    f32x16 acc[5] = {};
    bf16x8 f[3][6];
#define K2_GL(s, k) do { f[s][5] = *(const bf16x8*)(hp + (k) * 1024); _Pragma("unroll") for (int u = 0; u < 5; ++u) f[s][u] = *(const bf16x8*)(wp + (u * 108 + (k)) * 1024); SBAR(); } while (0)
#define K2_GM(s) do { _Pragma("unroll") for (int u = 0; u < 5; ++u) acc[u] = MFMA16(f[s][u], f[s][5], acc[u]); SBAR(); } while (0)
    K2_GL(0, 0); K2_GL(1, 1);
    RAW_BAR();
    for (int k = 0; k < 27; k += 3) {
      K2_GL(2, k + 2); K2_GM(0);
      if (k < 24) K2_GL(0, k + 3);
      K2_GM(1);
      if (k < 24) K2_GL(1, k + 4);
      K2_GM(2);
      if (k == 6 || k == 15) RAW_BAR();
    }
    u32x2 xv[5], bv[5];
#pragma unroll
    for (int u = 0; u < 5; ++u) {
      const int n = 160 * nb + 32 * u + 8 * i4 + 4 * hh;
      xv[u] = *(const u32x2*)(x + (32 * mt + r) * 1280 + n * 2); bv[u] = *(const u32x2*)(b2 + n * 2);
    }
    const lds_ptr red = lds + K2_RED + lane * 16;
    f32x4 own[5];
#pragma unroll
    for (int d = 0; d < 4; ++d) {
#pragma unroll
      for (int u = 0; u < 5; ++u) {
        const f32x4 q = {acc[u][4 * d], acc[u][4 * d + 1], acc[u][4 * d + 2], acc[u][4 * d + 3]};
        if (d == i4) own[u] = q; else stl<f32x4>(red + ((d * 3 + ((i4 - d - 1) & 3)) * 5 + u) * 1024, q);
      }
    }
    LGKM_BAR();
    char* pd = ws + WS_PART + (size_t)((mt * 4 + nb) * 4 + i4) * 5 * 1024 + lane * 16;
#pragma unroll
    for (int u = 0; u < 5; ++u) {
      f32x4 q = own[u];
#pragma unroll
      for (int s = 0; s < 3; ++s) q += ldl<f32x4>(red + ((i4 * 3 + s) * 5 + u) * 1024);
      q[0] += blo(xv[u][0]) + blo(bv[u][0]); q[1] += bhi(xv[u][0]) + bhi(bv[u][0]);
      q[2] += blo(xv[u][1]) + blo(bv[u][1]); q[3] += bhi(xv[u][1]) + bhi(bv[u][1]);
      *(f32x4*)(pd + u * 1024) = q;
    }
  }
}

constexpr int K3_T = 40960, K3_LDS = K3_T + 2 * 32 * 656;

__global__ __launch_bounds__(640, 3) void k3(char* ws, char* out) {
  extern __shared__ __attribute__((aligned(16))) char smem[];
  const lds_ptr lds = (lds_ptr)smem;
  const unsigned lds0 = (unsigned)(uintptr_t)smem;
  const int lane = threadIdx.x & 63, w = __builtin_amdgcn_readfirstlane(threadIdx.x >> 6), r = lane & 31, hh = lane >> 5;
  const int bid = blockIdx.x, xcd = bid & 7, j = bid >> 3;
  const int mt = 16 * (xcd >> 1) + (j >> 1), nb = 2 * (xcd & 1) + (j & 1);
  const int u = w % 5, kh = w / 5;
#pragma unroll
  for (int i = 0; i < 4; ++i) glds16(ws + WS_OI + (size_t)(mt * 40 + w + 10 * i) * 1024 + lane * 16, lds0 + (w + 10 * i) * 1024);
  f32x16 acc = {};
  if (kh == 0) {
#pragma unroll
    for (int i = 0; i < 4; ++i) {
      const f32x4 q = *(const f32x4*)(ws + WS_PART + (size_t)(((mt * 4 + nb) * 4 + i) * 5 + u) * 1024 + lane * 16);
      acc[4 * i] = q[0]; acc[4 * i + 1] = q[1]; acc[4 * i + 2] = q[2]; acc[4 * i + 3] = q[3];
    }
  }
  bf16x8 wf[20];
  const char* wp = ws + WS_WOI + (size_t)((nb * 5 + u) * 40 + 20 * kh) * 1024 + lane * 16;
#pragma unroll
  for (int k = 0; k < 20; ++k) wf[k] = *(const bf16x8*)(wp + k * 1024);
  SBAR();
  WAIT_BAR(20);
  const lds_ptr op = lds + kh * 20480 + lane * 16;
#pragma unroll
  for (int k = 0; k < 20; ++k) acc = MFMA16(wf[k], ldl<bf16x8>(op + k * 1024), acc);
#pragma unroll
  for (int i = 0; i < 4; ++i)
    stl<f32x4>(lds + K3_T + (kh * 32 + r) * 656 + (32 * u + 8 * i + 4 * hh) * 4, f32x4{acc[4 * i], acc[4 * i + 1], acc[4 * i + 2], acc[4 * i + 3]});
  LGKM_BAR();
  const int m = threadIdx.x / 20, c = threadIdx.x % 20;
  const lds_ptr tp = lds + K3_T + m * 656 + c * 32;
  const f32x4 a = ldl<f32x4>(tp) + ldl<f32x4>(tp + 32 * 656), b = ldl<f32x4>(tp + 16) + ldl<f32x4>(tp + 32 * 656 + 16);
  *(u32x4*)(out + (32 * mt + m) * 1280 + (160 * nb + 8 * c) * 2) = u32x4{cvtpk(a[0], a[1]), cvtpk(a[2], a[3]), cvtpk(b[0], b[1]), cvtpk(b[2], b[3])};
}

extern "C" void kernel_launch(void* const* d_in, const int* in_sizes, int n_in, void* d_out, int out_size, void* d_ws, size_t ws_size,
                              hipStream_t stream) {
  const char* x = (const char*)d_in[0]; const char* wqkv = (const char*)d_in[1]; const char* wo = (const char*)d_in[2];
  const char* w1 = (const char*)d_in[4]; const char* b1 = (const char*)d_in[5]; const char* w2 = (const char*)d_in[6]; const char* b2 = (const char*)d_in[7];
  char* ws = (char*)d_ws;
  (void)hipFuncSetAttribute((const void*)k1, hipFuncAttributeMaxDynamicSharedMemorySize, K1_LDS);
  (void)hipFuncSetAttribute((const void*)k2, hipFuncAttributeMaxDynamicSharedMemorySize, K2_LDS);
  (void)hipFuncSetAttribute((const void*)k3, hipFuncAttributeMaxDynamicSharedMemorySize, K3_LDS);
  k1<<<256, 512, K1_LDS, stream>>>(x, wqkv, w1, b1, w2, wo, ws);
  k2<<<256, 768, K2_LDS, stream>>>(x, b2, ws);
  k3<<<256, 640, K3_LDS, stream>>>(ws, (char*)d_out);
}
```

```cpp
#include <hip/hip_runtime.h>
#include <hip/hip_bf16.h>
#include <stdint.h>
#pragma clang diagnostic ignored "-Winline-asm"

using bf16x8 = __attribute__((ext_vector_type(8))) short;
using s16x4 = __attribute__((ext_vector_type(4))) short;
using f32x16 = __attribute__((ext_vector_type(16))) float;
using f32x4 = __attribute__((ext_vector_type(4))) float;
using f32x2 = __attribute__((ext_vector_type(2))) float;
using u32x4 = __attribute__((ext_vector_type(4))) unsigned;
using u32x2 = __attribute__((ext_vector_type(2))) unsigned;
typedef __attribute__((address_space(3))) char* lds_ptr;

#define DEVI __device__ __forceinline__
#define SBAR() __builtin_amdgcn_sched_barrier(0)
#define PIN(x) asm volatile("" : "+v"(x))
#define MFMA16(a, b, c) __builtin_amdgcn_mfma_f32_32x32x16_bf16(a, b, c, 0, 0, 0)
#define MFMA8(a, b, c) __builtin_amdgcn_mfma_f32_32x32x8bf16_1k(a, b, c, 0, 0, 0)
#define MFMAS(a, b, c) __builtin_amdgcn_mfma_f32_16x16x32_bf16(a, b, c, 0, 0, 0)
#define WAIT_BAR(N) asm volatile("s_waitcnt vmcnt(" #N ") lgkmcnt(0)\n\ts_barrier" ::: "memory")
#define LGKM_BAR() asm volatile("s_waitcnt lgkmcnt(0)\n\ts_barrier" ::: "memory")
#define RAW_BAR() asm volatile("s_barrier" ::: "memory")

#define PROF_DECL
#define STAMP(i)
#define PROF_END(kid)

constexpr int SEQ = 2048, DM = 640, FF = 1728;
constexpr int QT = 1280, KVT = 2624;
constexpr size_t WS_QI = 0;
constexpr int KVH = 66 * KVT;
constexpr size_t WS_KVI = WS_QI + 32 * 64 * QT;
constexpr size_t WS_HI = WS_KVI + 32 * KVH;
constexpr size_t WS_W2I = WS_HI + 64 * 108 * 1024;
constexpr size_t WS_WOI = WS_W2I + 20 * 108 * 1024;
constexpr size_t WS_OI = WS_WOI + 20 * 40 * 1024;
constexpr size_t WS_PART = WS_OI + 64 * 40 * 1024;
constexpr float LOG2E = 1.4426950408889634f;
constexpr float QSCALE = 0.22360679774997896f * LOG2E;
constexpr float GC1 = -2.f * 0.7978845608028654f * LOG2E, GC2 = GC1 * 0.044715f;

__host__ __device__ constexpr int recpos(int t) { return t < 2 ? t : 2 + ((t - 2) & 7) + (((t - 2) & 24) << 1) + (((t - 2) & 32) >> 2); }
typedef __bf16 bf16x2_t __attribute__((ext_vector_type(2)));
DEVI unsigned cvtpk(float lo, float hi) { return __builtin_bit_cast(unsigned, __builtin_convertvector(f32x2{lo, hi}, bf16x2_t)); }
DEVI void glds16(const void* g, unsigned lds_base) {
  unsigned sv;
  asm volatile("s_mov_b32 %0, m0\n\ts_mov_b32 m0, %2\n\ts_nop 0\n\tglobal_load_lds_dwordx4 %1, off\n\ts_mov_b32 m0, %0" : "=&s"(sv) : "v"(g), "s"(lds_base) : "memory");
}
DEVI void glds16s(const void* sbase, unsigned voff, unsigned lds_base) {
  asm volatile("s_mov_b32 m0, %2\n\ts_nop 0\n\tglobal_load_lds_dwordx4 %0, %1" :: "v"(voff), "s"(sbase), "s"(lds_base) : "m0", "memory");
}
DEVI s16x4 ldtr(lds_ptr p) { return __builtin_amdgcn_ds_read_tr16_b64_v4i16((__attribute__((address_space(3))) s16x4*)p); }
DEVI bf16x8 ldtr2(lds_ptr p, int off2) { s16x4 a = ldtr(p), b = ldtr(p + off2); return __builtin_shufflevector(a, b, 0, 1, 2, 3, 4, 5, 6, 7); }
template <typename T> DEVI T ldl(lds_ptr p) { return *(__attribute__((address_space(3))) T*)p; }
template <typename T> DEVI void stl(lds_ptr p, T v) { *(__attribute__((address_space(3))) T*)p = v; }
DEVI float blo(unsigned d) { return __uint_as_float(d << 16); }
DEVI float bhi(unsigned d) { return __uint_as_float(d & 0xffff0000u); }
DEVI u32x4 pack8(const float* v) { return u32x4{cvtpk(v[0], v[1]), cvtpk(v[2], v[3]), cvtpk(v[4], v[5]), cvtpk(v[6], v[7])}; }
DEVI float gelu(float u) {
  float z = u * __builtin_fmaf(GC2, u * u, GC1);
  return u * __builtin_amdgcn_rcpf(1.f + __builtin_amdgcn_exp2f(z));
}

constexpr int K1_STG = 49152, K1_SP = 3 * K1_STG, K1_LDS = K1_SP + 16384;
constexpr int K1_RS = K1_SP, K1_CS = K1_SP + 1024;

struct K1Frag { bf16x8 x[2], w[2]; };

template <bool VF>
DEVI void k1_main(f32x16 (&acc)[2][2], f32x4 (&cs)[2], lds_ptr lds, int xa0, int wa0, bf16x8 sel) {
  lds_ptr xp[4], wp[2];
#pragma unroll
  for (int p = 0; p < 4; ++p) xp[p] = lds + (xa0 ^ (p * 32));
  wp[0] = lds + 16384 + wa0; wp[1] = lds + 16384 + (wa0 ^ 4096);
  K1Frag A, B;
  B.x[0] = B.x[1] = B.w[0] = B.w[1] = bf16x8{};
#define K1_LD(F, p) do { F.x[0] = ldl<bf16x8>(xp[p]); F.w[0] = ldtr2(wp[(p) & 1] + ((p) >> 1) * 8192, 1024); F.x[1] = ldl<bf16x8>(xp[p] + 4096); \
    F.w[1] = ldtr2(wp[(p) & 1] + ((p) >> 1) * 8192 + 16384, 1024); } while (0)
#define K1_MM(F) do { \
    if (VF) { acc[0][0] = MFMA16(F.x[0], F.w[0], acc[0][0]); acc[0][1] = MFMA16(F.x[1], F.w[0], acc[0][1]); } \
    else { acc[0][0] = MFMA16(F.w[0], F.x[0], acc[0][0]); acc[0][1] = MFMA16(F.w[0], F.x[1], acc[0][1]); } \
    acc[1][0] = MFMA16(F.w[1], F.x[0], acc[1][0]); acc[1][1] = MFMA16(F.w[1], F.x[1], acc[1][1]); } while (0)
#define K1_CSUM(F) do { cs[0] = MFMAS(F.w[0], sel, cs[0]); cs[1] = MFMAS(F.w[1], sel, cs[1]); } while (0)
  for (int s = 0; s < 10; ++s) {
    LGKM_BAR();
    K1_LD(A, 0); SBAR();
    K1_MM(B); SBAR();
    K1_LD(B, 1); SBAR();
    K1_MM(A); K1_CSUM(A); SBAR();
    K1_LD(A, 2); SBAR();
    K1_MM(B); SBAR();
    K1_LD(B, 3); SBAR();
    K1_MM(A); K1_CSUM(A); SBAR();
    const int dl = (s % 3 == 2) ? -2 * K1_STG : K1_STG;
#pragma unroll
    for (int p = 0; p < 4; ++p) xp[p] += dl;
    wp[0] += dl; wp[1] += dl;
  }
  K1_MM(B);
}

__global__ __launch_bounds__(768, 3) void k1(const char* __restrict__ x, const char* __restrict__ wqkv, const char* __restrict__ w1,
                                             const char* __restrict__ b1, const char* __restrict__ w2, const char* __restrict__ wo, char* __restrict__ ws) {
  extern __shared__ __attribute__((aligned(16))) char smem[];
  const lds_ptr lds = (lds_ptr)smem;
  const unsigned lds0 = (unsigned)(uintptr_t)smem;
  const int lane = threadIdx.x & 63, w = __builtin_amdgcn_readfirstlane(threadIdx.x >> 6), r = lane & 31, hh = lane >> 5;
  const int bid = blockIdx.x, x8 = bid & 7, j = bid >> 3;
  const int q4 = (lane & 15) >> 2, cb = 32 * ((lane >> 4) & 1) + 8 * (lane & 3);
  PROF_DECL STAMP(0);
  if (j >= 30) {
    const int hid = 2 * x8 + (j - 30), gw = 12 * hid + w;
#pragma unroll
    for (int rd = 0; rd < 2; ++rd) {
      char* dst[4]; int ust[4];
#pragma unroll
      for (int i = 0; i < 4; ++i) {
        int b = gw + 192 * (4 * rd + i); b = b < 1480 ? b : gw;
        const bool isw2 = b < 1080;
        const int bb = isw2 ? b : b - 1080, v = bb % 10, ks = bb / 10, slot = lane >> 3;
        const int row0 = isw2 ? 16 * ks + slot : (ks < 32 ? 20 * ks + slot : 20 * (4 * (ks - 32) + 2 * (slot >> 2)) + 16 + (slot & 3));
        const int rstep = (isw2 || ks < 32) ? 8 : 20;
        const char* src = (isw2 ? w2 : wo) + row0 * 1280 + v * 128 + (lane & 7) * 16;
        glds16(src, lds0 + w * 8192 + i * 2048); glds16(src + rstep * 1280, lds0 + w * 8192 + i * 2048 + 1024);
        const int u0 = 2 * v, u1 = 2 * v + 1;
        const int o0 = isw2 ? ((u0 / 5) * 108 + ks) * 5120 + (u0 % 5) * 1024 : (u0 * 40 + ks) * 1024;
        const int o1 = isw2 ? ((u1 / 5) * 108 + ks) * 5120 + (u1 % 5) * 1024 : (u1 * 40 + ks) * 1024;
        ust[i] = o1 - o0;
        dst[i] = ws + (isw2 ? WS_W2I : WS_WOI) + o0 + lane * 16;
      }
      if (rd == 0) {
        const int t = hid * 768 + threadIdx.x;
        if (t < 8192) *(u32x4*)(ws + WS_KVI + (size_t)(t >> 8) * KVH + recpos(((t >> 2) & 63) + 1 + ((t >> 1) & 1)) * KVT + 1280 + (t & 3) * 336 + 320) =
            u32x4{0x3f803f80u, 0x3f803f80u, 0x3f803f80u, 0x3f803f80u};
      }
      asm volatile("s_waitcnt vmcnt(0)" ::: "memory");
      const lds_ptr rp = lds + w * 8192 + (4 * hh + q4) * 128 + cb;
#pragma unroll
      for (int i = 0; i < 4; ++i) {
        const bf16x8 a = ldtr2(rp + i * 2048, 1024), b = ldtr2(rp + i * 2048 + 64, 1024);
        *(bf16x8*)dst[i] = a; *(bf16x8*)(dst[i] + ust[i]) = b;
      }
      asm volatile("s_waitcnt lgkmcnt(0)" ::: "memory");
    }
    PROF_END(0);
    return;
  }
  const int L = 30 * x8 + j, mhalf = L >= 120, Lr = L - 120 * mhalf, n = Lr >> 3, mg = 8 * mhalf + (Lr & 7);
  if (w >= 8) {
    const int lw = w - 8;
    unsigned xo[4], qo[4], mo[4];
    const int ch = (lane & 15) ^ ((lane >> 4) << 2), chm = n >= 13 ? (ch & 7) : ch;
#pragma unroll
    for (int i = 0; i < 4; ++i) {
      xo[i] = (32 * lw + 8 * i + (lane >> 3)) * 1280 + (((lane & 7) ^ ((4 * i + (lane >> 4)) & 7)) * 16);
      qo[i] = (16 * lw + 4 * i + (lane >> 4)) * 3840 + ch * 16;
      mo[i] = (16 * lw + 4 * i + (lane >> 4)) * 3456 + chm * 16;
    }
    const char* xb = x + mg * (128 * 1280);
    const char* qb = wqkv + n * 256;
    const char* mb = w1 + (n < 13 ? n : 13) * 256;
    unsigned nb = lds0 + lw * 4096;
#pragma unroll
    for (int s = 0; s < 2; ++s) {
#pragma unroll
      for (int i = 0; i < 4; ++i) { glds16s(xb, xo[i], nb + i * 1024); glds16s(qb, qo[i], nb + 16384 + i * 1024); glds16s(mb, mo[i], nb + 32768 + i * 1024); }
      xb += 128; qb += 64 * 3840; mb += 64 * 3456; nb += K1_STG;
    }
    lds_ptr sp[4];
#pragma unroll
    for (int i = 0; i < 4; ++i) sp[i] = lds + (32 * lw + r) * 128 + (((2 * i + hh) ^ ((r >> 1) & 7)) * 16);
    float s0 = 0.f, s1 = 0.f, q0 = 0.f, q1 = 0.f;
    STAMP(1);
    for (int s = 0; s < 10; ++s) {
      if (s < 9) WAIT_BAR(12); else WAIT_BAR(0);
      if (s == 0) STAMP(2);
      u32x4 d[4];
#pragma unroll
      for (int i = 0; i < 4; ++i) d[i] = ldl<u32x4>(sp[i]);
      SBAR();
#define K1_ST(v) do { const float lo = blo(v), hi = bhi(v); \
        asm("v_add_f32 %0, %0, %2\n\tv_fmac_f32 %1, %2, %2" : "+v"(s0), "+v"(q0) : "v"(lo)); asm("v_add_f32 %0, %0, %2\n\tv_fmac_f32 %1, %2, %2" : "+v"(s1), "+v"(q1) : "v"(hi)); } while (0)
      if (s < 8) {
#pragma unroll
        for (int i = 0; i < 4; ++i) {
          glds16s(xb, xo[i], nb + i * 1024); K1_ST(d[i][0]); SBAR();
          glds16s(qb, qo[i], nb + 16384 + i * 1024); K1_ST(d[i][1]); SBAR();
          glds16s(mb, mo[i], nb + 32768 + i * 1024); K1_ST(d[i][2]); K1_ST(d[i][3]); SBAR();
        }
        xb += 128; qb += 64 * 3840; mb += 64 * 3456;
        nb = (s % 3 == 0) ? nb - 2 * K1_STG : nb + K1_STG;
      } else {
#pragma unroll
        for (int i = 0; i < 4; ++i) { K1_ST(d[i][0]); K1_ST(d[i][1]); K1_ST(d[i][2]); K1_ST(d[i][3]); }
      }
      const int dl = (s % 3 == 2) ? -2 * K1_STG : K1_STG;
#pragma unroll
      for (int i = 0; i < 4; ++i) sp[i] += dl;
    }
    STAMP(3);
    s0 += s1; q0 += q1;
    auto a = __builtin_amdgcn_permlane32_swap(__float_as_uint(s0), __float_as_uint(q0), false, false);
    const float t = __uint_as_float(a[0]) + __uint_as_float(a[1]);
    auto b = __builtin_amdgcn_permlane32_swap(__float_as_uint(t), __float_as_uint(t), false, false);
    const float mean = __uint_as_float(b[0]) * (1.f / 640.f);
    const float var = __builtin_fmaxf(__builtin_fmaf(-mean, mean, __uint_as_float(b[1]) * (1.f / 640.f)), 0.f);
    const float rstd = __builtin_amdgcn_rsqf(var + 1e-5f);
    stl<float>(lds + K1_RS + hh * 512 + (32 * lw + r) * 4, hh ? -mean * rstd : rstd);
    LGKM_BAR();
    STAMP(4);
    PROF_END(0);
    return;
  }
  const int mh = w & 1, np = w >> 1;
  const int fb = 128 * n + 32 * np;
  const bool mlp_ok = fb < FF;
  u32x2 bias[4];
#pragma unroll
  for (int i = 0; i < 4; ++i) bias[i] = *(const u32x2*)(b1 + (mlp_ok ? fb + 8 * i + 4 * hh : 0) * 2);
  const int xa0 = (64 * mh + r) * 128 + (((2 * mh + hh) ^ ((r >> 1) & 7)) * 16);
  const int wa0 = (16 * mh + 8 * hh + q4) * 256 + ((64 * np + cb) ^ (q4 << 6));
  const short one = ((lane & 15) == ((lane >> 4) & 1)) ? (short)0x3f80 : (short)0;
  const bf16x8 sel = {one, one, one, one, one, one, one, one};
  f32x16 acc[2][2] = {};
  f32x4 cs[2] = {};
  STAMP(1);
  if (n < 10) k1_main<false>(acc, cs, lds, xa0, wa0, sel);
  else k1_main<true>(acc, cs, lds, xa0, wa0, sel);
  STAMP(2);
  PIN(bias[0]); PIN(bias[1]); PIN(bias[2]); PIN(bias[3]);
  const lds_ptr CS = lds + K1_CS + np * 256;
  if ((lane & 15) < 2) {
    stl<f32x4>(CS + mh * 1024 + (16 * (lane & 1) + 4 * (lane >> 4)) * 4, cs[0]);
    stl<f32x4>(CS + mh * 1024 + 128 + (16 * (lane & 1) + 4 * (lane >> 4)) * 4, cs[1]);
  }
  LGKM_BAR();
  const int mt0 = 4 * mg + 2 * mh;
  const lds_ptr RS = lds + K1_RS + 256 * mh;
  float rs[2], nm[2];
#pragma unroll
  for (int t = 0; t < 2; ++t) { rs[t] = ldl<float>(RS + (32 * t + r) * 4); nm[t] = ldl<float>(RS + 512 + (32 * t + r) * 4); }
  if (n < 10) {
    const bool isq = n < 5;
    const float sc = isq ? QSCALE : 1.f;
    const int tb = isq ? QT : KVT, hb = isq ? 64 * QT : KVH;
    char* base = ws + (isq ? WS_QI + mt0 * QT : WS_KVI + recpos(mt0) * KVT);
    const int q0 = 32 * (isq ? n : n - 5) + 8 * np + hh;
#pragma unroll
    for (int g = 0; g < 4; ++g) {
      const f32x4 c = ldl<f32x4>(CS + (8 * g + 4 * hh) * 4) + ldl<f32x4>(CS + 1024 + (8 * g + 4 * hh) * 4);
      const int qd = q0 + 2 * g, hd = (qd * 205) >> 10, dq = qd - 5 * hd;
      char* dst = base + hd * hb + (dq < 4 ? (dq & 1) * 512 + r * 16 + (dq >> 1) * 8 : 1024 + r * 8);
#pragma unroll
      for (int t = 0; t < 2; ++t) {
        const float a = rs[t] * sc, b = nm[t] * sc;
        float v[4];
#pragma unroll
        for (int e = 0; e < 4; ++e) v[e] = __builtin_fmaf(a, acc[0][t][4 * g + e], b * c[e]);
        *(u32x2*)(dst + t * tb) = u32x2{cvtpk(v[0], v[1]), cvtpk(v[2], v[3])};
      }
    }
  } else {
    const float c = ldl<float>(CS + r * 4) + ldl<float>(CS + 1024 + r * 4);
    const int col = 128 * (n - 10) + 32 * np + r, hd = (col * 3277) >> 16, d = col - 20 * hd;
    char* dst = ws + WS_KVI + hd * KVH + 1280 + hh * 336 + d * 16;
    const int ro[3] = {recpos(mt0 + 1) * KVT, recpos(mt0 + 2) * KVT, recpos(mt0 + 2) * KVT + KVT};
#pragma unroll
    for (int t = 0; t < 2; ++t) {
      float v[16];
#pragma unroll
      for (int i = 0; i < 4; ++i) {
        const f32x4 a = ldl<f32x4>(RS + (32 * t + 8 * i + 4 * hh) * 4), b = ldl<f32x4>(RS + 512 + (32 * t + 8 * i + 4 * hh) * 4);
#pragma unroll
        for (int e = 0; e < 4; ++e) v[4 * i + e] = __builtin_fmaf(a[e], acc[0][t][4 * i + e], b[e] * c);
      }
      *(u32x4*)(dst + ro[t]) = pack8(v); *(u32x4*)(dst + ro[t + 1] + 672) = pack8(v + 8);
    }
  }
  STAMP(3);
  if (mlp_ok) {
    f32x4 c4[4];
#pragma unroll
    for (int i = 0; i < 4; ++i) c4[i] = ldl<f32x4>(CS + 128 + (8 * i + 4 * hh) * 4) + ldl<f32x4>(CS + 1024 + 128 + (8 * i + 4 * hh) * 4);
    char* hdst = ws + WS_HI + (size_t)(mt0 * 108 + 8 * n + 2 * np) * 1024 + lane * 16;
#pragma unroll
    for (int t = 0; t < 2; ++t) {
      float v[16];
#pragma unroll
      for (int i = 0; i < 16; ++i) {
        const unsigned bd = bias[i >> 2][(i >> 1) & 1];
        v[i] = gelu(__builtin_fmaf(rs[t], acc[1][t][i], __builtin_fmaf(nm[t], c4[i >> 2][i & 3], (i & 1) ? bhi(bd) : blo(bd))));
      }
      *(u32x4*)(hdst + t * 108 * 1024) = pack8(v);
      *(u32x4*)(hdst + t * 108 * 1024 + 1024) = pack8(v + 8);
    }
  }
  STAMP(4);
  PROF_END(0);
}

constexpr int K2_CH = 16 * KVT, K2_RED = 2 * K2_CH, K2_LDS = K2_RED + 61440;

DEVI float max16(const f32x16& s) {
  float a = __builtin_fmaxf(__builtin_fmaxf(s[0], s[1]), s[2]), b = __builtin_fmaxf(__builtin_fmaxf(s[3], s[4]), s[5]);
  a = __builtin_fmaxf(__builtin_fmaxf(a, s[6]), s[7]); b = __builtin_fmaxf(__builtin_fmaxf(b, s[8]), s[9]);
  a = __builtin_fmaxf(__builtin_fmaxf(a, s[10]), s[11]); b = __builtin_fmaxf(__builtin_fmaxf(b, s[12]), s[13]);
  a = __builtin_fmaxf(__builtin_fmaxf(a, s[14]), s[15]); a = __builtin_fmaxf(a, b);
  auto t = __builtin_amdgcn_permlane32_swap(__float_as_uint(a), __float_as_uint(a), false, false);
  return __builtin_fmaxf(__uint_as_float(t[0]), __uint_as_float(t[1]));
}

__global__ __launch_bounds__(768, 3) void k2(const char* __restrict__ x, const char* __restrict__ b2, char* __restrict__ ws) {
  extern __shared__ __attribute__((aligned(16))) char smem[];
  const lds_ptr lds = (lds_ptr)smem;
  const unsigned lds0 = (unsigned)(uintptr_t)smem;
  const int lane = threadIdx.x & 63, w = __builtin_amdgcn_readfirstlane(threadIdx.x >> 6), r = lane & 31, hh = lane >> 5;
  const int bid = blockIdx.x;
  const unsigned l16 = lane * 16;
  PROF_DECL STAMP(0);
  if (w < 8) {
    const int h = 4 * (bid & 7) + ((bid >> 3) & 3), kh = w >> 2, mt = 8 * (bid >> 5) + 2 * (w & 3);
    const char* kv = ws + WS_KVI + (size_t)h * KVH;
    const char* qp = ws + WS_QI + (size_t)(h * 64 + mt) * QT;
    const char* k0p = kv + (kh ? recpos(32) * KVT : 0);
    const unsigned r8 = 1024 + r * 8, vo = 1280 + hh * 336 + (r < 20 ? r : 20) * 16;
    bf16x8 qmA, qmB, kmf, km0, km, v0; s16x4 qrA, qrB, krf, kr0, kr;
    const char* src = kv + 2 * KVT + w * 1024;
#define K2_FILL5(c) do { _Pragma("unroll") for (int j = 0; j < 5; ++j) glds16s(src + (c) * K2_CH + j * 8192, l16, lds0 + ((c) & 1) * K2_CH + w * 1024 + j * 8192); } while (0)
#define K2_FILL1(c) do { if (w == 0) glds16s(src + (c) * K2_CH + 40960, l16, lds0 + ((c) & 1) * K2_CH + 40960); } while (0)
#define K2_FILL(c) do { K2_FILL5(c); K2_FILL1(c); } while (0)
    K2_FILL1(0);
    asm volatile("global_load_dwordx4 %0, %11, %13\n\tglobal_load_dwordx2 %1, %12, %13\n\tglobal_load_dwordx4 %2, %11, %14\n\tglobal_load_dwordx2 %3, %12, %14\n\t"
                 "global_load_dwordx4 %4, %11, %13 offset:1280\n\tglobal_load_dwordx2 %5, %12, %13 offset:1280\n\t"
                 "global_load_dwordx4 %6, %11, %15\n\tglobal_load_dwordx2 %7, %12, %15\n\t"
                 "global_load_dwordx2 %8, %12, %15 offset:2624\n\tglobal_load_dwordx4 %9, %11, %15 offset:2624\n\tglobal_load_dwordx4 %10, %16, %15 offset:2624"
                 : "=&v"(qmA), "=&v"(qrA), "=&v"(kmf), "=&v"(krf), "=&v"(qmB), "=&v"(qrB), "=&v"(km0), "=&v"(kr0), "=&v"(kr), "=&v"(km), "=&v"(v0)
                 : "v"(l16), "v"(r8), "s"(qp), "s"(kv), "s"(k0p), "v"(vo) : "memory");
    K2_FILL5(0);
    asm volatile("s_waitcnt vmcnt(10)" : "+v"(qmA), "+v"(qrA), "+v"(kmf), "+v"(krf), "+v"(qmB), "+v"(qrB) :: "memory");
    if (hh) { qrA = s16x4{}; qrB = s16x4{}; }
    f32x16 sA, sB, nA, nB;
    {
      sA = MFMA8(krf, qrA, f32x16{}); sB = MFMA8(krf, qrB, f32x16{}); sA = MFMA16(kmf, qmA, sA); sB = MFMA16(kmf, qmB, sB);
      const unsigned mA = cvtpk(-max16(sA), 0.f), mB = cvtpk(-max16(sB), 0.f);
      if (hh) { qrA[0] = (short)mA; qrB[0] = (short)mB; }
    }
    SBAR();
    asm volatile("s_waitcnt vmcnt(5)" : "+v"(km0), "+v"(kr0), "+v"(kr), "+v"(km), "+v"(v0) :: "memory");
    if (hh) { kr0 = s16x4{0x3f80, 0x3f80, 0x3f80, 0x3f80}; kr = kr0; }
    sA = MFMA8(kr0, qrA, f32x16{}); sB = MFMA8(kr0, qrB, f32x16{}); sA = MFMA16(km0, qmA, sA); sB = MFMA16(km0, qmB, sB);
    f32x16 oA = {}, oB = {};
    bf16x8 v1 = {};
    u32x4 pA0, pB0, pA1 = {}, pB1 = {};
    lds_ptr kmp = lds + kh * 8 * KVT + l16, krp = lds + kh * 8 * KVT + (hh ? 1600 : r8), vp = lds + kh * 8 * KVT + vo;
    SBAR();
    STAMP(1);
    WAIT_BAR(0);
    STAMP(2);
    K2_FILL(1);
#define K2_EXP4(S, i, P, j) do { P[j] = cvtpk(__builtin_amdgcn_exp2f(S[i]), __builtin_amdgcn_exp2f(S[i + 1])); P[j + 1] = cvtpk(__builtin_amdgcn_exp2f(S[i + 2]), __builtin_amdgcn_exp2f(S[i + 3])); } while (0)
#define BC(p) __builtin_bit_cast(bf16x8, p)
#define K2_TILE(SA, SB, NA, NB, off) do { \
      NA = MFMA8(kr, qrA, f32x16{}); SBAR(); K2_EXP4(SA, 0, pA0, 0); SBAR(); \
      NB = MFMA8(kr, qrB, f32x16{}); SBAR(); kr = ldl<s16x4>(krp + (off)); K2_EXP4(SA, 4, pA0, 2); SBAR(); \
      oA = MFMA16(v1, BC(pA1), oA); SBAR(); K2_EXP4(SB, 0, pB0, 0); SBAR(); \
      oB = MFMA16(v1, BC(pB1), oB); SBAR(); v1 = ldl<bf16x8>(vp + (off) + 672); K2_EXP4(SB, 4, pB0, 2); SBAR(); \
      NA = MFMA16(km, qmA, NA); SBAR(); K2_EXP4(SA, 8, pA1, 0); SBAR(); \
      NB = MFMA16(km, qmB, NB); SBAR(); km = ldl<bf16x8>(kmp + (off)); K2_EXP4(SA, 12, pA1, 2); SBAR(); \
      oA = MFMA16(v0, BC(pA0), oA); SBAR(); K2_EXP4(SB, 8, pB1, 0); SBAR(); \
      oB = MFMA16(v0, BC(pB0), oB); SBAR(); v0 = ldl<bf16x8>(vp + (off)); K2_EXP4(SB, 12, pB1, 2); SBAR(); } while (0)
#define APRIO(x)
    for (int c = 0; c < 4; ++c) {
      APRIO(2);
      K2_TILE(sA, sB, nA, nB, 0 * KVT); K2_TILE(nA, nB, sA, sB, 1 * KVT);
      K2_TILE(sA, sB, nA, nB, 2 * KVT); K2_TILE(nA, nB, sA, sB, 3 * KVT);
      APRIO(1);
      K2_TILE(sA, sB, nA, nB, 4 * KVT); K2_TILE(nA, nB, sA, sB, 5 * KVT);
      K2_TILE(sA, sB, nA, nB, 6 * KVT);
      APRIO(0);
      K2_TILE(nA, nB, sA, sB, 7 * KVT);
      PIN(pA1); PIN(pB1);
      if (c < 3) {
        WAIT_BAR(0);
        if (c < 2) K2_FILL(c + 2);
        const int dl = (c & 1) ? -K2_CH : K2_CH;
        kmp += dl; krp += dl; vp += dl;
      }
    }
    oA = MFMA16(v1, BC(pA1), oA); oB = MFMA16(v1, BC(pB1), oB);
    STAMP(3);
    f32x16 o = kh ? oB : oA;
    {
      const f32x16 g = kh ? oA : oB;
      const lds_ptr xw = lds + w * 3072 + l16, xr = lds + (w ^ 4) * 3072 + l16;
#pragma unroll
      for (int i = 0; i < 3; ++i) stl<f32x4>(xw + i * 1024, f32x4{g[4 * i], g[4 * i + 1], g[4 * i + 2], g[4 * i + 3]});
      LGKM_BAR();
#pragma unroll
      for (int i = 0; i < 3; ++i) { const f32x4 q = ldl<f32x4>(xr + i * 1024); o[4 * i] += q[0]; o[4 * i + 1] += q[1]; o[4 * i + 2] += q[2]; o[4 * i + 3] += q[3]; }
    }
    auto lt = __builtin_amdgcn_permlane32_swap(__float_as_uint(o[8]), __float_as_uint(o[8]), false, false);
    float l = __uint_as_float(lt[1]);
    float om = 0.f;
#pragma unroll
    for (int i = 0; i < 12; ++i) om = __builtin_fmaxf(om, __builtin_fabsf(o[i]));
    const bool bad = !(l >= 0x1p-30f && l <= 0x1p60f && om <= 0x1p126f);
    if (__builtin_amdgcn_ballot_w64(bad) != 0) {
      const bf16x8 qm = kh ? qmB : qmA;
      s16x4 qr = kh ? qrB : qrA;
      if (hh) qr = s16x4{};
      float m = -__builtin_inff();
      o = f32x16{};
      for (int t = 0; t < 64; ++t) {
        const char* tp = kv + recpos(t) * KVT;
        const bf16x8 a = *(const bf16x8*)(tp + l16), va = *(const bf16x8*)(kv + recpos(t + 1) * KVT + vo), vb = *(const bf16x8*)(kv + recpos(t + 2) * KVT + vo + 672);
        const s16x4 b = *(const s16x4*)(tp + r8);
        f32x16 s = MFMA8(b, qr, f32x16{}); s = MFMA16(a, qm, s);
        const float mn = __builtin_fmaxf(m, max16(s)), al = __builtin_amdgcn_exp2f(m - mn);
        m = mn;
        float p[16];
#pragma unroll
        for (int i = 0; i < 16; ++i) { p[i] = __builtin_amdgcn_exp2f(s[i] - mn); o[i] *= al; }
        o = MFMA16(va, BC(pack8(p)), o);
        o = MFMA16(vb, BC(pack8(p + 8)), o);
      }
      lt = __builtin_amdgcn_permlane32_swap(__float_as_uint(o[8]), __float_as_uint(o[8]), false, false);
      l = __uint_as_float(lt[1]);
    }
    const float il = __builtin_amdgcn_rcpf(l);
    float v[12];
#pragma unroll
    for (int i = 0; i < 12; ++i) v[i] = o[i] * il;
    char* od = ws + WS_OI + (size_t)(mt + kh) * 40 * 1024;
    *(u32x4*)(od + h * 1024 + lane * 16) = pack8(v);
    if (!hh) *(u32x2*)(od + (32 + (h >> 2)) * 1024 + ((h >> 1) & 1) * 512 + r * 16 + (h & 1) * 8) = u32x2{cvtpk(v[8], v[9]), cvtpk(v[10], v[11])};
  } else {
#define GSLEEP 1
    const int i4 = w - 8, xcd = bid & 7, j = bid >> 3;
    const int mt = 16 * (xcd >> 1) + (j >> 1), nb = 2 * (xcd & 1) + (j & 1);
    const char* hp = ws + WS_HI + (size_t)(mt * 108 + 27 * i4) * 1024;
    const char* wp = ws + WS_W2I + (size_t)(nb * 108 + 27 * i4) * 5120 + 2048;
    f32x16 acc[5] = {};
    bf16x8 f[3][6];
#define K2_LW(s, u) asm volatile("global_load_dwordx4 %0, %1, %2 offset:%3" : "=v"(f[s][u]) : "v"(l16), "s"(wp), "n"((u) * 1024 - 2048) : "memory")
#define K2_LH(s) do { asm volatile("global_load_dwordx4 %0, %1, %2" : "=v"(f[s][5]) : "v"(l16), "s"(hp) : "memory"); hp += 1024; wp += 5120; } while (0)
#define K2_GW(s, N) asm volatile("s_waitcnt vmcnt(" #N ")" : "+v"(f[s][0]), "+v"(f[s][1]), "+v"(f[s][2]), "+v"(f[s][3]), "+v"(f[s][4]), "+v"(f[s][5]) :: "memory")
#define K2_G1(s, u, LD) do { acc[u] = MFMA16(f[s][u], f[s][5], acc[u]); SBAR(); if (LD) K2_LW(s, u); if (GSLEEP) __builtin_amdgcn_s_sleep(GSLEEP); SBAR(); } while (0)
#define K2_GS(s, LD) do { K2_G1(s, 0, LD); K2_G1(s, 1, LD); K2_G1(s, 2, LD); K2_G1(s, 3, LD); K2_G1(s, 4, LD); if (LD) K2_LH(s); } while (0)
    RAW_BAR();
#pragma unroll
    for (int s = 0; s < 3; ++s) { K2_LW(s, 0); K2_LW(s, 1); K2_LW(s, 2); K2_LW(s, 3); K2_LW(s, 4); K2_LH(s); }
    STAMP(1);
    for (int k = 0; k < 8; ++k) {
      K2_GW(0, 12); K2_GS(0, true);
      K2_GW(1, 12); K2_GS(1, true);
      K2_GW(2, 12); K2_GS(2, true);
      if (k == 2) STAMP(2);
      if (k == 2 || k == 5) RAW_BAR();
    }
    K2_GW(0, 12); K2_GS(0, false);
    K2_GW(1, 6); K2_GS(1, false);
    K2_GW(2, 0); K2_GS(2, false);
    STAMP(3);
    u32x2 xv[5], bv[5];
#pragma unroll
    for (int u = 0; u < 5; ++u) {
      const int n = 160 * nb + 32 * u + 8 * i4 + 4 * hh;
      xv[u] = *(const u32x2*)(x + (32 * mt + r) * 1280 + n * 2); bv[u] = *(const u32x2*)(b2 + n * 2);
    }
    const lds_ptr red = lds + K2_RED + lane * 16;
    f32x4 own[5];
#pragma unroll
    for (int d = 0; d < 4; ++d) {
#pragma unroll
      for (int u = 0; u < 5; ++u) {
        const f32x4 q = {acc[u][4 * d], acc[u][4 * d + 1], acc[u][4 * d + 2], acc[u][4 * d + 3]};
        if (d == i4) own[u] = q; else stl<f32x4>(red + ((d * 3 + ((i4 - d - 1) & 3)) * 5 + u) * 1024, q);
      }
    }
    LGKM_BAR();
    char* pd = ws + WS_PART + (size_t)((mt * 4 + nb) * 4 + i4) * 5 * 1024 + lane * 16;
#pragma unroll
    for (int u = 0; u < 5; ++u) {
      f32x4 q = own[u];
#pragma unroll
      for (int s = 0; s < 3; ++s) q += ldl<f32x4>(red + ((i4 * 3 + s) * 5 + u) * 1024);
      q[0] += blo(xv[u][0]) + blo(bv[u][0]); q[1] += bhi(xv[u][0]) + bhi(bv[u][0]);
      q[2] += blo(xv[u][1]) + blo(bv[u][1]); q[3] += bhi(xv[u][1]) + bhi(bv[u][1]);
      *(f32x4*)(pd + u * 1024) = q;
    }
  }
  STAMP(4);
  PROF_END(1);
}

constexpr int K3_T = 40960, K3_LDS = K3_T + 2 * 32 * 656;

__global__ __launch_bounds__(640, 3) void k3(char* ws, char* out) {
  extern __shared__ __attribute__((aligned(16))) char smem[];
  const lds_ptr lds = (lds_ptr)smem;
  const unsigned lds0 = (unsigned)(uintptr_t)smem;
  const int lane = threadIdx.x & 63, w = __builtin_amdgcn_readfirstlane(threadIdx.x >> 6), r = lane & 31, hh = lane >> 5;
  const int bid = blockIdx.x, xcd = bid & 7, j = bid >> 3;
  const int mt = 16 * (xcd >> 1) + (j >> 1), nb = 2 * (xcd & 1) + (j & 1);
  const int u = w % 5, kh = w / 5;
  PROF_DECL STAMP(0);
#pragma unroll
  for (int i = 0; i < 4; ++i) glds16(ws + WS_OI + (size_t)(mt * 40 + w + 10 * i) * 1024 + lane * 16, lds0 + (w + 10 * i) * 1024);
  f32x16 acc = {};
  if (kh == 0) {
#pragma unroll
    for (int i = 0; i < 4; ++i) {
      const f32x4 q = *(const f32x4*)(ws + WS_PART + (size_t)(((mt * 4 + nb) * 4 + i) * 5 + u) * 1024 + lane * 16);
      acc[4 * i] = q[0]; acc[4 * i + 1] = q[1]; acc[4 * i + 2] = q[2]; acc[4 * i + 3] = q[3];
    }
  }
  bf16x8 wf[20];
  const char* wp = ws + WS_WOI + (size_t)((nb * 5 + u) * 40 + 20 * kh) * 1024 + lane * 16;
#pragma unroll
  for (int k = 0; k < 20; ++k) wf[k] = *(const bf16x8*)(wp + k * 1024);
  SBAR();
  STAMP(1);
  WAIT_BAR(20);
  STAMP(2);
  const lds_ptr op = lds + kh * 20480 + lane * 16;
#pragma unroll
  for (int k = 0; k < 20; ++k) acc = MFMA16(wf[k], ldl<bf16x8>(op + k * 1024), acc);
#pragma unroll
  for (int i = 0; i < 4; ++i)
    stl<f32x4>(lds + K3_T + (kh * 32 + r) * 656 + (32 * u + 8 * i + 4 * hh) * 4, f32x4{acc[4 * i], acc[4 * i + 1], acc[4 * i + 2], acc[4 * i + 3]});
  STAMP(3);
  LGKM_BAR();
  const int m = threadIdx.x / 20, c = threadIdx.x % 20;
  const lds_ptr tp = lds + K3_T + m * 656 + c * 32;
  const f32x4 a = ldl<f32x4>(tp) + ldl<f32x4>(tp + 32 * 656), b = ldl<f32x4>(tp + 16) + ldl<f32x4>(tp + 32 * 656 + 16);
  *(u32x4*)(out + (32 * mt + m) * 1280 + (160 * nb + 8 * c) * 2) = u32x4{cvtpk(a[0], a[1]), cvtpk(a[2], a[3]), cvtpk(b[0], b[1]), cvtpk(b[2], b[3])};
  STAMP(4);
  PROF_END(2);
}

extern "C" void kernel_launch(void* const* d_in, const int* in_sizes, int n_in, void* d_out, int out_size, void* d_ws, size_t ws_size,
                              hipStream_t stream) {
  const char* x = (const char*)d_in[0]; const char* wqkv = (const char*)d_in[1]; const char* wo = (const char*)d_in[2];
  const char* w1 = (const char*)d_in[4]; const char* b1 = (const char*)d_in[5]; const char* w2 = (const char*)d_in[6]; const char* b2 = (const char*)d_in[7];
  char* ws = (char*)d_ws;
  (void)hipFuncSetAttribute((const void*)k1, hipFuncAttributeMaxDynamicSharedMemorySize, K1_LDS);
  (void)hipFuncSetAttribute((const void*)k2, hipFuncAttributeMaxDynamicSharedMemorySize, K2_LDS);
  (void)hipFuncSetAttribute((const void*)k3, hipFuncAttributeMaxDynamicSharedMemorySize, K3_LDS);
  k1<<<256, 768, K1_LDS, stream>>>(x, wqkv, w1, b1, w2, wo, ws);
  k2<<<256, 768, K2_LDS, stream>>>(x, b2, ws);
  k3<<<256, 640, K3_LDS, stream>>>(ws, (char*)d_out);
}
```

```cpp
#define PROF 3
#include <cstdio>
#include <vector>
#include <algorithm>
#include <hip/hip_runtime.h>
#include <hip/hip_bf16.h>
#include <stdint.h>
#pragma clang diagnostic ignored "-Winline-asm"

using bf16x8 = __attribute__((ext_vector_type(8))) short;
using s16x4 = __attribute__((ext_vector_type(4))) short;
using f32x16 = __attribute__((ext_vector_type(16))) float;
using f32x4 = __attribute__((ext_vector_type(4))) float;
using f32x2 = __attribute__((ext_vector_type(2))) float;
using u32x4 = __attribute__((ext_vector_type(4))) unsigned;
using u32x2 = __attribute__((ext_vector_type(2))) unsigned;
typedef __attribute__((address_space(3))) char* lds_ptr;

#define DEVI __device__ __forceinline__
#define SBAR() __builtin_amdgcn_sched_barrier(0)
#define PIN(x) asm volatile("" : "+v"(x))
#define MFMA16(a, b, c) __builtin_amdgcn_mfma_f32_32x32x16_bf16(a, b, c, 0, 0, 0)
#define MFMA8(a, b, c) __builtin_amdgcn_mfma_f32_32x32x8bf16_1k(a, b, c, 0, 0, 0)
#define MFMAS(a, b, c) __builtin_amdgcn_mfma_f32_16x16x32_bf16(a, b, c, 0, 0, 0)
#define WAIT_BAR(N) asm volatile("s_waitcnt vmcnt(" #N ") lgkmcnt(0)\n\ts_barrier" ::: "memory")
#define LGKM_BAR() asm volatile("s_waitcnt lgkmcnt(0)\n\ts_barrier" ::: "memory")
#define RAW_BAR() asm volatile("s_barrier" ::: "memory")

constexpr size_t WS_PROF = 64u << 20;
#define PROF_DECL unsigned long long ts_[8] = {}; { unsigned long long t_; asm volatile("s_memrealtime %0\n\ts_waitcnt lgkmcnt(0)" : "=s"(t_) :: "memory"); ts_[6] = t_; }
#define STAMP(i) do { SBAR(); unsigned long long t_; asm volatile("s_memtime %0\n\ts_waitcnt lgkmcnt(0)" : "=s"(t_) :: "memory"); ts_[i] = t_; SBAR(); } while (0)
#define PROF_END(kid) do { unsigned long long t_; asm volatile("s_waitcnt vmcnt(0)\n\ts_memtime %0\n\ts_waitcnt lgkmcnt(0)" : "=s"(t_) :: "memory"); ts_[5] = t_; asm volatile("s_memrealtime %0\n\ts_waitcnt lgkmcnt(0)" : "=s"(t_) :: "memory"); ts_[7] = t_; \
    if ((threadIdx.x & 63) == 0) for (int i_ = 0; i_ < 8; ++i_) ((unsigned long long*)(ws + WS_PROF))[(((kid) * 256 + blockIdx.x) * 16 + (threadIdx.x >> 6)) * 8 + i_] = ts_[i_]; } while (0)

constexpr int SEQ = 2048, DM = 640, FF = 1728;
constexpr int QT = 1280, KVT = 2624;
constexpr size_t WS_QI = 0;
constexpr int KVH = 66 * KVT;
constexpr size_t WS_KVI = WS_QI + 32 * 64 * QT;
constexpr size_t WS_HI = WS_KVI + 32 * KVH;
constexpr size_t WS_W2I = WS_HI + 64 * 108 * 1024;
constexpr size_t WS_WOI = WS_W2I + 20 * 108 * 1024;
constexpr size_t WS_OI = WS_WOI + 20 * 40 * 1024;
constexpr size_t WS_PART = WS_OI + 64 * 40 * 1024;
constexpr float LOG2E = 1.4426950408889634f;
constexpr float QSCALE = 0.22360679774997896f * LOG2E;
constexpr float GC1 = -2.f * 0.7978845608028654f * LOG2E, GC2 = GC1 * 0.044715f;

__host__ __device__ constexpr int recpos(int t) { return t < 2 ? t : 2 + ((t - 2) & 7) + (((t - 2) & 24) << 1) + (((t - 2) & 32) >> 2); }
typedef __bf16 bf16x2_t __attribute__((ext_vector_type(2)));
DEVI unsigned cvtpk(float lo, float hi) { return __builtin_bit_cast(unsigned, __builtin_convertvector(f32x2{lo, hi}, bf16x2_t)); }
DEVI void glds16(const void* g, unsigned lds_base) {
  unsigned sv;
  asm volatile("s_mov_b32 %0, m0\n\ts_mov_b32 m0, %2\n\ts_nop 0\n\tglobal_load_lds_dwordx4 %1, off\n\ts_mov_b32 m0, %0" : "=&s"(sv) : "v"(g), "s"(lds_base) : "memory");
}
DEVI void glds16s(const void* sbase, unsigned voff, unsigned lds_base) {
  asm volatile("s_mov_b32 m0, %2\n\ts_nop 0\n\tglobal_load_lds_dwordx4 %0, %1" :: "v"(voff), "s"(sbase), "s"(lds_base) : "m0", "memory");
}
DEVI s16x4 ldtr(lds_ptr p) { return __builtin_amdgcn_ds_read_tr16_b64_v4i16((__attribute__((address_space(3))) s16x4*)p); }
DEVI bf16x8 ldtr2(lds_ptr p, int off2) { s16x4 a = ldtr(p), b = ldtr(p + off2); return __builtin_shufflevector(a, b, 0, 1, 2, 3, 4, 5, 6, 7); }
template <typename T> DEVI T ldl(lds_ptr p) { return *(__attribute__((address_space(3))) T*)p; }
template <typename T> DEVI void stl(lds_ptr p, T v) { *(__attribute__((address_space(3))) T*)p = v; }
DEVI float blo(unsigned d) { return __uint_as_float(d << 16); }
DEVI float bhi(unsigned d) { return __uint_as_float(d & 0xffff0000u); }
DEVI u32x4 pack8(const float* v) { return u32x4{cvtpk(v[0], v[1]), cvtpk(v[2], v[3]), cvtpk(v[4], v[5]), cvtpk(v[6], v[7])}; }
DEVI float gelu(float u) {
  float z = u * __builtin_fmaf(GC2, u * u, GC1);
  return u * __builtin_amdgcn_rcpf(1.f + __builtin_amdgcn_exp2f(z));
}

constexpr int K1_STG = 49152, K1_SP = 3 * K1_STG, K1_LDS = K1_SP + 16384;
constexpr int K1_RS = K1_SP, K1_CS = K1_SP + 1024;

struct K1Frag { bf16x8 x[2], w[2]; };

template <bool VF>
DEVI void k1_main(f32x16 (&acc)[2][2], f32x4 (&cs)[2], lds_ptr lds, int xa0, int wa0, bf16x8 sel) {
  lds_ptr xp[4], wp[2];
#pragma unroll
  for (int p = 0; p < 4; ++p) xp[p] = lds + (xa0 ^ (p * 32));
  wp[0] = lds + 16384 + wa0; wp[1] = lds + 16384 + (wa0 ^ 4096);
  K1Frag A, B;
  B.x[0] = B.x[1] = B.w[0] = B.w[1] = bf16x8{};
#define K1_LD(F, p) do { F.x[0] = ldl<bf16x8>(xp[p]); F.w[0] = ldtr2(wp[(p) & 1] + ((p) >> 1) * 8192, 1024); F.x[1] = ldl<bf16x8>(xp[p] + 4096); \
    F.w[1] = ldtr2(wp[(p) & 1] + ((p) >> 1) * 8192 + 16384, 1024); } while (0)
#define K1_MM(F) do { \
    if (VF) { acc[0][0] = MFMA16(F.x[0], F.w[0], acc[0][0]); acc[0][1] = MFMA16(F.x[1], F.w[0], acc[0][1]); } \
    else { acc[0][0] = MFMA16(F.w[0], F.x[0], acc[0][0]); acc[0][1] = MFMA16(F.w[0], F.x[1], acc[0][1]); } \
    acc[1][0] = MFMA16(F.w[1], F.x[0], acc[1][0]); acc[1][1] = MFMA16(F.w[1], F.x[1], acc[1][1]); } while (0)
#define K1_CSUM(F) do { cs[0] = MFMAS(F.w[0], sel, cs[0]); cs[1] = MFMAS(F.w[1], sel, cs[1]); } while (0)
  for (int s = 0; s < 10; ++s) {
    LGKM_BAR();
    K1_LD(A, 0); SBAR();
    K1_MM(B); SBAR();
    K1_LD(B, 1); SBAR();
    K1_MM(A); K1_CSUM(A); SBAR();
    K1_LD(A, 2); SBAR();
    K1_MM(B); SBAR();
    K1_LD(B, 3); SBAR();
    K1_MM(A); K1_CSUM(A); SBAR();
    const int dl = (s % 3 == 2) ? -2 * K1_STG : K1_STG;
#pragma unroll
    for (int p = 0; p < 4; ++p) xp[p] += dl;
    wp[0] += dl; wp[1] += dl;
  }
  K1_MM(B);
}

__global__ __launch_bounds__(768, 3) void k1(const char* __restrict__ x, const char* __restrict__ wqkv, const char* __restrict__ w1,
                                             const char* __restrict__ b1, const char* __restrict__ w2, const char* __restrict__ wo, char* __restrict__ ws) {
  extern __shared__ __attribute__((aligned(16))) char smem[];
  const lds_ptr lds = (lds_ptr)smem;
  const unsigned lds0 = (unsigned)(uintptr_t)smem;
  const int lane = threadIdx.x & 63, w = __builtin_amdgcn_readfirstlane(threadIdx.x >> 6), r = lane & 31, hh = lane >> 5;
  const int bid = blockIdx.x, x8 = bid & 7, j = bid >> 3;
  const int q4 = (lane & 15) >> 2, cb = 32 * ((lane >> 4) & 1) + 8 * (lane & 3);
  PROF_DECL STAMP(0);
  if (j >= 30) {
    const int hid = 2 * x8 + (j - 30), gw = 12 * hid + w;
#pragma unroll
    for (int rd = 0; rd < 2; ++rd) {
      char* dst[4]; int ust[4];
#pragma unroll
      for (int i = 0; i < 4; ++i) {
        int b = gw + 192 * (4 * rd + i); b = b < 1480 ? b : gw;
        const bool isw2 = b < 1080;
        const int bb = isw2 ? b : b - 1080, v = bb % 10, ks = bb / 10, slot = lane >> 3;
        const int row0 = isw2 ? 16 * ks + slot : (ks < 32 ? 20 * ks + slot : 20 * (4 * (ks - 32) + 2 * (slot >> 2)) + 16 + (slot & 3));
        const int rstep = (isw2 || ks < 32) ? 8 : 20;
        const char* src = (isw2 ? w2 : wo) + row0 * 1280 + v * 128 + (lane & 7) * 16;
        glds16(src, lds0 + w * 8192 + i * 2048); glds16(src + rstep * 1280, lds0 + w * 8192 + i * 2048 + 1024);
        const int u0 = 2 * v, u1 = 2 * v + 1;
        const int o0 = isw2 ? ((u0 / 5) * 108 + ks) * 5120 + (u0 % 5) * 1024 : (u0 * 40 + ks) * 1024;
        const int o1 = isw2 ? ((u1 / 5) * 108 + ks) * 5120 + (u1 % 5) * 1024 : (u1 * 40 + ks) * 1024;
        ust[i] = o1 - o0;
        dst[i] = ws + (isw2 ? WS_W2I : WS_WOI) + o0 + lane * 16;
      }
      if (rd == 0) {
        const int t = hid * 768 + threadIdx.x;
        if (t < 8192) *(u32x4*)(ws + WS_KVI + (size_t)(t >> 8) * KVH + recpos(((t >> 2) & 63) + 1 + ((t >> 1) & 1)) * KVT + 1280 + (t & 3) * 336 + 320) =
            u32x4{0x3f803f80u, 0x3f803f80u, 0x3f803f80u, 0x3f803f80u};
      }
      asm volatile("s_waitcnt vmcnt(0)" ::: "memory");
      const lds_ptr rp = lds + w * 8192 + (4 * hh + q4) * 128 + cb;
#pragma unroll
      for (int i = 0; i < 4; ++i) {
        const bf16x8 a = ldtr2(rp + i * 2048, 1024), b = ldtr2(rp + i * 2048 + 64, 1024);
        *(bf16x8*)dst[i] = a; *(bf16x8*)(dst[i] + ust[i]) = b;
      }
      asm volatile("s_waitcnt lgkmcnt(0)" ::: "memory");
    }
    PROF_END(0);
    return;
  }
  const int L = 30 * x8 + j, mhalf = L >= 120, Lr = L - 120 * mhalf, n = Lr >> 3, mg = 8 * mhalf + (Lr & 7);
  if (w >= 8) {
    const int lw = w - 8;
    unsigned xo[4], qo[4], mo[4];
    const int ch = (lane & 15) ^ ((lane >> 4) << 2), chm = n >= 13 ? (ch & 7) : ch;
#pragma unroll
    for (int i = 0; i < 4; ++i) {
      xo[i] = (32 * lw + 8 * i + (lane >> 3)) * 1280 + (((lane & 7) ^ ((4 * i + (lane >> 4)) & 7)) * 16);
      qo[i] = (16 * lw + 4 * i + (lane >> 4)) * 3840 + ch * 16;
      mo[i] = (16 * lw + 4 * i + (lane >> 4)) * 3456 + chm * 16;
    }
    const char* xb = x + mg * (128 * 1280);
    const char* qb = wqkv + n * 256;
    const char* mb = w1 + (n < 13 ? n : 13) * 256;
    unsigned nb = lds0 + lw * 4096;
#pragma unroll
    for (int s = 0; s < 2; ++s) {
#pragma unroll
      for (int i = 0; i < 4; ++i) { glds16s(xb, xo[i], nb + i * 1024); glds16s(qb, qo[i], nb + 16384 + i * 1024); glds16s(mb, mo[i], nb + 32768 + i * 1024); }
      xb += 128; qb += 64 * 3840; mb += 64 * 3456; nb += K1_STG;
    }
    lds_ptr sp[4];
#pragma unroll
    for (int i = 0; i < 4; ++i) sp[i] = lds + (32 * lw + r) * 128 + (((2 * i + hh) ^ ((r >> 1) & 7)) * 16);
    float s0 = 0.f, s1 = 0.f, q0 = 0.f, q1 = 0.f;
    STAMP(1);
    for (int s = 0; s < 10; ++s) {
      if (s < 9) WAIT_BAR(12); else WAIT_BAR(0);
      if (s == 0) STAMP(2);
      u32x4 d[4];
#pragma unroll
      for (int i = 0; i < 4; ++i) d[i] = ldl<u32x4>(sp[i]);
      SBAR();
#define K1_ST(v) do { const float lo = blo(v), hi = bhi(v); \
        asm("v_add_f32 %0, %0, %2\n\tv_fmac_f32 %1, %2, %2" : "+v"(s0), "+v"(q0) : "v"(lo)); asm("v_add_f32 %0, %0, %2\n\tv_fmac_f32 %1, %2, %2" : "+v"(s1), "+v"(q1) : "v"(hi)); } while (0)
      if (s < 8) {
#pragma unroll
        for (int i = 0; i < 4; ++i) {
          glds16s(xb, xo[i], nb + i * 1024); K1_ST(d[i][0]); SBAR();
          glds16s(qb, qo[i], nb + 16384 + i * 1024); K1_ST(d[i][1]); SBAR();
          glds16s(mb, mo[i], nb + 32768 + i * 1024); K1_ST(d[i][2]); K1_ST(d[i][3]); SBAR();
        }
        xb += 128; qb += 64 * 3840; mb += 64 * 3456;
        nb = (s % 3 == 0) ? nb - 2 * K1_STG : nb + K1_STG;
      } else {
#pragma unroll
        for (int i = 0; i < 4; ++i) { K1_ST(d[i][0]); K1_ST(d[i][1]); K1_ST(d[i][2]); K1_ST(d[i][3]); }
      }
      const int dl = (s % 3 == 2) ? -2 * K1_STG : K1_STG;
#pragma unroll
      for (int i = 0; i < 4; ++i) sp[i] += dl;
    }
    STAMP(3);
    s0 += s1; q0 += q1;
    auto a = __builtin_amdgcn_permlane32_swap(__float_as_uint(s0), __float_as_uint(q0), false, false);
    const float t = __uint_as_float(a[0]) + __uint_as_float(a[1]);
    auto b = __builtin_amdgcn_permlane32_swap(__float_as_uint(t), __float_as_uint(t), false, false);
    const float mean = __uint_as_float(b[0]) * (1.f / 640.f);
    const float var = __builtin_fmaxf(__builtin_fmaf(-mean, mean, __uint_as_float(b[1]) * (1.f / 640.f)), 0.f);
    const float rstd = __builtin_amdgcn_rsqf(var + 1e-5f);
    stl<float>(lds + K1_RS + hh * 512 + (32 * lw + r) * 4, hh ? -mean * rstd : rstd);
    LGKM_BAR();
    STAMP(4);
    PROF_END(0);
    return;
  }
  const int mh = w & 1, np = w >> 1;
  const int fb = 128 * n + 32 * np;
  const bool mlp_ok = fb < FF;
  u32x2 bias[4];
#pragma unroll
  for (int i = 0; i < 4; ++i) bias[i] = *(const u32x2*)(b1 + (mlp_ok ? fb + 8 * i + 4 * hh : 0) * 2);
  const int xa0 = (64 * mh + r) * 128 + (((2 * mh + hh) ^ ((r >> 1) & 7)) * 16);
  const int wa0 = (16 * mh + 8 * hh + q4) * 256 + ((64 * np + cb) ^ (q4 << 6));
  const short one = ((lane & 15) == ((lane >> 4) & 1)) ? (short)0x3f80 : (short)0;
  const bf16x8 sel = {one, one, one, one, one, one, one, one};
  f32x16 acc[2][2] = {};
  f32x4 cs[2] = {};
  STAMP(1);
  if (n < 10) k1_main<false>(acc, cs, lds, xa0, wa0, sel);
  else k1_main<true>(acc, cs, lds, xa0, wa0, sel);
  STAMP(2);
  PIN(bias[0]); PIN(bias[1]); PIN(bias[2]); PIN(bias[3]);
  const lds_ptr CS = lds + K1_CS + np * 256;
  if ((lane & 15) < 2) {
    stl<f32x4>(CS + mh * 1024 + (16 * (lane & 1) + 4 * (lane >> 4)) * 4, cs[0]);
    stl<f32x4>(CS + mh * 1024 + 128 + (16 * (lane & 1) + 4 * (lane >> 4)) * 4, cs[1]);
  }
  LGKM_BAR();
  const int mt0 = 4 * mg + 2 * mh;
  const lds_ptr RS = lds + K1_RS + 256 * mh;
  float rs[2], nm[2];
#pragma unroll
  for (int t = 0; t < 2; ++t) { rs[t] = ldl<float>(RS + (32 * t + r) * 4); nm[t] = ldl<float>(RS + 512 + (32 * t + r) * 4); }
  if (n < 10) {
    const bool isq = n < 5;
    const float sc = isq ? QSCALE : 1.f;
    const int tb = isq ? QT : KVT, hb = isq ? 64 * QT : KVH;
    char* base = ws + (isq ? WS_QI + mt0 * QT : WS_KVI + recpos(mt0) * KVT);
    const int q0 = 32 * (isq ? n : n - 5) + 8 * np + hh;
#pragma unroll
    for (int g = 0; g < 4; ++g) {
      const f32x4 c = ldl<f32x4>(CS + (8 * g + 4 * hh) * 4) + ldl<f32x4>(CS + 1024 + (8 * g + 4 * hh) * 4);
      const int qd = q0 + 2 * g, hd = (qd * 205) >> 10, dq = qd - 5 * hd;
      char* dst = base + hd * hb + (dq < 4 ? (dq & 1) * 512 + r * 16 + (dq >> 1) * 8 : 1024 + r * 8);
#pragma unroll
      for (int t = 0; t < 2; ++t) {
        const float a = rs[t] * sc, b = nm[t] * sc;
        float v[4];
#pragma unroll
        for (int e = 0; e < 4; ++e) v[e] = __builtin_fmaf(a, acc[0][t][4 * g + e], b * c[e]);
        *(u32x2*)(dst + t * tb) = u32x2{cvtpk(v[0], v[1]), cvtpk(v[2], v[3])};
      }
    }
  } else {
    const float c = ldl<float>(CS + r * 4) + ldl<float>(CS + 1024 + r * 4);
    const int col = 128 * (n - 10) + 32 * np + r, hd = (col * 3277) >> 16, d = col - 20 * hd;
    char* dst = ws + WS_KVI + hd * KVH + 1280 + hh * 336 + d * 16;
    const int ro[3] = {recpos(mt0 + 1) * KVT, recpos(mt0 + 2) * KVT, recpos(mt0 + 2) * KVT + KVT};
#pragma unroll
    for (int t = 0; t < 2; ++t) {
      float v[16];
#pragma unroll
      for (int i = 0; i < 4; ++i) {
        const f32x4 a = ldl<f32x4>(RS + (32 * t + 8 * i + 4 * hh) * 4), b = ldl<f32x4>(RS + 512 + (32 * t + 8 * i + 4 * hh) * 4);
#pragma unroll
        for (int e = 0; e < 4; ++e) v[4 * i + e] = __builtin_fmaf(a[e], acc[0][t][4 * i + e], b[e] * c);
      }
      *(u32x4*)(dst + ro[t]) = pack8(v); *(u32x4*)(dst + ro[t + 1] + 672) = pack8(v + 8);
    }
  }
  STAMP(3);
  if (mlp_ok) {
    f32x4 c4[4];
#pragma unroll
    for (int i = 0; i < 4; ++i) c4[i] = ldl<f32x4>(CS + 128 + (8 * i + 4 * hh) * 4) + ldl<f32x4>(CS + 1024 + 128 + (8 * i + 4 * hh) * 4);
    char* hdst = ws + WS_HI + (size_t)(mt0 * 108 + 8 * n + 2 * np) * 1024 + lane * 16;
#pragma unroll
    for (int t = 0; t < 2; ++t) {
      float v[16];
#pragma unroll
      for (int i = 0; i < 16; ++i) {
        const unsigned bd = bias[i >> 2][(i >> 1) & 1];
        v[i] = gelu(__builtin_fmaf(rs[t], acc[1][t][i], __builtin_fmaf(nm[t], c4[i >> 2][i & 3], (i & 1) ? bhi(bd) : blo(bd))));
      }
      *(u32x4*)(hdst + t * 108 * 1024) = pack8(v);
      *(u32x4*)(hdst + t * 108 * 1024 + 1024) = pack8(v + 8);
    }
  }
  STAMP(4);
  PROF_END(0);
}

constexpr int K2_CH = 16 * KVT, K2_RED = 2 * K2_CH, K2_LDS = K2_RED + 61440;

DEVI float max16(const f32x16& s) {
  float a = __builtin_fmaxf(__builtin_fmaxf(s[0], s[1]), s[2]), b = __builtin_fmaxf(__builtin_fmaxf(s[3], s[4]), s[5]);
  a = __builtin_fmaxf(__builtin_fmaxf(a, s[6]), s[7]); b = __builtin_fmaxf(__builtin_fmaxf(b, s[8]), s[9]);
  a = __builtin_fmaxf(__builtin_fmaxf(a, s[10]), s[11]); b = __builtin_fmaxf(__builtin_fmaxf(b, s[12]), s[13]);
  a = __builtin_fmaxf(__builtin_fmaxf(a, s[14]), s[15]); a = __builtin_fmaxf(a, b);
  auto t = __builtin_amdgcn_permlane32_swap(__float_as_uint(a), __float_as_uint(a), false, false);
  return __builtin_fmaxf(__uint_as_float(t[0]), __uint_as_float(t[1]));
}

__global__ __launch_bounds__(768, 3) void k2(const char* __restrict__ x, const char* __restrict__ b2, char* __restrict__ ws) {
  extern __shared__ __attribute__((aligned(16))) char smem[];
  const lds_ptr lds = (lds_ptr)smem;
  const unsigned lds0 = (unsigned)(uintptr_t)smem;
  const int lane = threadIdx.x & 63, w = __builtin_amdgcn_readfirstlane(threadIdx.x >> 6), r = lane & 31, hh = lane >> 5;
  const int bid = blockIdx.x;
  const unsigned l16 = lane * 16;
  PROF_DECL STAMP(0);
  if (w < 8) {
    const int h = 4 * (bid & 7) + ((bid >> 3) & 3), kh = w >> 2, mt = 8 * (bid >> 5) + 2 * (w & 3);
    const char* kv = ws + WS_KVI + (size_t)h * KVH;
    const char* qp = ws + WS_QI + (size_t)(h * 64 + mt) * QT;
    const char* k0p = kv + (kh ? recpos(32) * KVT : 0);
    const unsigned r8 = 1024 + r * 8, vo = 1280 + hh * 336 + (r < 20 ? r : 20) * 16;
    bf16x8 qmA, qmB, kmf, km0, km, v0; s16x4 qrA, qrB, krf, kr0, kr;
    const char* src = kv + 2 * KVT + w * 1024;
#define K2_FILL5(c) do { _Pragma("unroll") for (int j = 0; j < 5; ++j) glds16s(src + (c) * K2_CH + j * 8192, l16, lds0 + ((c) & 1) * K2_CH + w * 1024 + j * 8192); } while (0)
#define K2_FILL1(c) do { if (w == 0) glds16s(src + (c) * K2_CH + 40960, l16, lds0 + ((c) & 1) * K2_CH + 40960); } while (0)
#define K2_FILL(c) do { K2_FILL5(c); K2_FILL1(c); } while (0)
    K2_FILL1(0);
    asm volatile("global_load_dwordx4 %0, %11, %13\n\tglobal_load_dwordx2 %1, %12, %13\n\tglobal_load_dwordx4 %2, %11, %14\n\tglobal_load_dwordx2 %3, %12, %14\n\t"
                 "global_load_dwordx4 %4, %11, %13 offset:1280\n\tglobal_load_dwordx2 %5, %12, %13 offset:1280\n\t"
                 "global_load_dwordx4 %6, %11, %15\n\tglobal_load_dwordx2 %7, %12, %15\n\t"
                 "global_load_dwordx2 %8, %12, %15 offset:2624\n\tglobal_load_dwordx4 %9, %11, %15 offset:2624\n\tglobal_load_dwordx4 %10, %16, %15 offset:2624"
                 : "=&v"(qmA), "=&v"(qrA), "=&v"(kmf), "=&v"(krf), "=&v"(qmB), "=&v"(qrB), "=&v"(km0), "=&v"(kr0), "=&v"(kr), "=&v"(km), "=&v"(v0)
                 : "v"(l16), "v"(r8), "s"(qp), "s"(kv), "s"(k0p), "v"(vo) : "memory");
    K2_FILL5(0);
    asm volatile("s_waitcnt vmcnt(10)" : "+v"(qmA), "+v"(qrA), "+v"(kmf), "+v"(krf), "+v"(qmB), "+v"(qrB) :: "memory");
    if (hh) { qrA = s16x4{}; qrB = s16x4{}; }
    f32x16 sA, sB, nA, nB;
    {
      sA = MFMA8(krf, qrA, f32x16{}); sB = MFMA8(krf, qrB, f32x16{}); sA = MFMA16(kmf, qmA, sA); sB = MFMA16(kmf, qmB, sB);
      const unsigned mA = cvtpk(-max16(sA), 0.f), mB = cvtpk(-max16(sB), 0.f);
      if (hh) { qrA[0] = (short)mA; qrB[0] = (short)mB; }
    }
    SBAR();
    asm volatile("s_waitcnt vmcnt(5)" : "+v"(km0), "+v"(kr0), "+v"(kr), "+v"(km), "+v"(v0) :: "memory");
    if (hh) { kr0 = s16x4{0x3f80, 0x3f80, 0x3f80, 0x3f80}; kr = kr0; }
    sA = MFMA8(kr0, qrA, f32x16{}); sB = MFMA8(kr0, qrB, f32x16{}); sA = MFMA16(km0, qmA, sA); sB = MFMA16(km0, qmB, sB);
    f32x16 oA = {}, oB = {};
    bf16x8 v1 = {};
    u32x4 pA0, pB0, pA1 = {}, pB1 = {};
    lds_ptr kmp = lds + kh * 8 * KVT + l16, krp = lds + kh * 8 * KVT + (hh ? 1600 : r8), vp = lds + kh * 8 * KVT + vo;
    SBAR();
    STAMP(1);
    WAIT_BAR(0);
    STAMP(2);
    K2_FILL(1);
#define K2_EXP4(S, i, P, j) do { P[j] = cvtpk(__builtin_amdgcn_exp2f(S[i]), __builtin_amdgcn_exp2f(S[i + 1])); P[j + 1] = cvtpk(__builtin_amdgcn_exp2f(S[i + 2]), __builtin_amdgcn_exp2f(S[i + 3])); } while (0)
#define BC(p) __builtin_bit_cast(bf16x8, p)
#define K2_TILE(SA, SB, NA, NB, off) do { \
      NA = MFMA8(kr, qrA, f32x16{}); SBAR(); K2_EXP4(SA, 0, pA0, 0); SBAR(); \
      NB = MFMA8(kr, qrB, f32x16{}); SBAR(); kr = ldl<s16x4>(krp + (off)); K2_EXP4(SA, 4, pA0, 2); SBAR(); \
      oA = MFMA16(v1, BC(pA1), oA); SBAR(); K2_EXP4(SB, 0, pB0, 0); SBAR(); \
      oB = MFMA16(v1, BC(pB1), oB); SBAR(); v1 = ldl<bf16x8>(vp + (off) + 672); K2_EXP4(SB, 4, pB0, 2); SBAR(); \
      NA = MFMA16(km, qmA, NA); SBAR(); K2_EXP4(SA, 8, pA1, 0); SBAR(); \
      NB = MFMA16(km, qmB, NB); SBAR(); km = ldl<bf16x8>(kmp + (off)); K2_EXP4(SA, 12, pA1, 2); SBAR(); \
      oA = MFMA16(v0, BC(pA0), oA); SBAR(); K2_EXP4(SB, 8, pB1, 0); SBAR(); \
      oB = MFMA16(v0, BC(pB0), oB); SBAR(); v0 = ldl<bf16x8>(vp + (off)); K2_EXP4(SB, 12, pB1, 2); SBAR(); } while (0)
#define APRIO(x)
    for (int c = 0; c < 4; ++c) {
      APRIO(2);
      K2_TILE(sA, sB, nA, nB, 0 * KVT); K2_TILE(nA, nB, sA, sB, 1 * KVT);
      K2_TILE(sA, sB, nA, nB, 2 * KVT); K2_TILE(nA, nB, sA, sB, 3 * KVT);
      APRIO(1);
      K2_TILE(sA, sB, nA, nB, 4 * KVT); K2_TILE(nA, nB, sA, sB, 5 * KVT);
      K2_TILE(sA, sB, nA, nB, 6 * KVT);
      APRIO(0);
      K2_TILE(nA, nB, sA, sB, 7 * KVT);
      PIN(pA1); PIN(pB1);
      if (c < 3) {
        WAIT_BAR(0);
        if (c < 2) K2_FILL(c + 2);
        const int dl = (c & 1) ? -K2_CH : K2_CH;
        kmp += dl; krp += dl; vp += dl;
      }
    }
    oA = MFMA16(v1, BC(pA1), oA); oB = MFMA16(v1, BC(pB1), oB);
    STAMP(3);
    f32x16 o = kh ? oB : oA;
    {
      const f32x16 g = kh ? oA : oB;
      const lds_ptr xw = lds + w * 3072 + l16, xr = lds + (w ^ 4) * 3072 + l16;
#pragma unroll
      for (int i = 0; i < 3; ++i) stl<f32x4>(xw + i * 1024, f32x4{g[4 * i], g[4 * i + 1], g[4 * i + 2], g[4 * i + 3]});
      LGKM_BAR();
#pragma unroll
      for (int i = 0; i < 3; ++i) { const f32x4 q = ldl<f32x4>(xr + i * 1024); o[4 * i] += q[0]; o[4 * i + 1] += q[1]; o[4 * i + 2] += q[2]; o[4 * i + 3] += q[3]; }
    }
    auto lt = __builtin_amdgcn_permlane32_swap(__float_as_uint(o[8]), __float_as_uint(o[8]), false, false);
    float l = __uint_as_float(lt[1]);
    float om = 0.f;
#pragma unroll
    for (int i = 0; i < 12; ++i) om = __builtin_fmaxf(om, __builtin_fabsf(o[i]));
    const bool bad = !(l >= 0x1p-30f && l <= 0x1p60f && om <= 0x1p126f);
    if (__builtin_amdgcn_ballot_w64(bad) != 0) {
      const bf16x8 qm = kh ? qmB : qmA;
      s16x4 qr = kh ? qrB : qrA;
      if (hh) qr = s16x4{};
      float m = -__builtin_inff();
      o = f32x16{};
      for (int t = 0; t < 64; ++t) {
        const char* tp = kv + recpos(t) * KVT;
        const bf16x8 a = *(const bf16x8*)(tp + l16), va = *(const bf16x8*)(kv + recpos(t + 1) * KVT + vo), vb = *(const bf16x8*)(kv + recpos(t + 2) * KVT + vo + 672);
        const s16x4 b = *(const s16x4*)(tp + r8);
        f32x16 s = MFMA8(b, qr, f32x16{}); s = MFMA16(a, qm, s);
        const float mn = __builtin_fmaxf(m, max16(s)), al = __builtin_amdgcn_exp2f(m - mn);
        m = mn;
        float p[16];
#pragma unroll
        for (int i = 0; i < 16; ++i) { p[i] = __builtin_amdgcn_exp2f(s[i] - mn); o[i] *= al; }
        o = MFMA16(va, BC(pack8(p)), o);
        o = MFMA16(vb, BC(pack8(p + 8)), o);
      }
      lt = __builtin_amdgcn_permlane32_swap(__float_as_uint(o[8]), __float_as_uint(o[8]), false, false);
      l = __uint_as_float(lt[1]);
    }
    const float il = __builtin_amdgcn_rcpf(l);
    float v[12];
#pragma unroll
    for (int i = 0; i < 12; ++i) v[i] = o[i] * il;
    char* od = ws + WS_OI + (size_t)(mt + kh) * 40 * 1024;
    *(u32x4*)(od + h * 1024 + lane * 16) = pack8(v);
    if (!hh) *(u32x2*)(od + (32 + (h >> 2)) * 1024 + ((h >> 1) & 1) * 512 + r * 16 + (h & 1) * 8) = u32x2{cvtpk(v[8], v[9]), cvtpk(v[10], v[11])};
  } else {
#define GSLEEP 1
    const int i4 = w - 8, xcd = bid & 7, j = bid >> 3;
    const int mt = 16 * (xcd >> 1) + (j >> 1), nb = 2 * (xcd & 1) + (j & 1);
    const char* hp = ws + WS_HI + (size_t)(mt * 108 + 27 * i4) * 1024;
    const char* wp = ws + WS_W2I + (size_t)(nb * 108 + 27 * i4) * 5120 + 2048;
    f32x16 acc[5] = {};
    bf16x8 f[3][6];
#define K2_LW(s, u) asm volatile("global_load_dwordx4 %0, %1, %2 offset:%3" : "=v"(f[s][u]) : "v"(l16), "s"(wp), "n"((u) * 1024 - 2048) : "memory")
#define K2_LH(s) do { asm volatile("global_load_dwordx4 %0, %1, %2" : "=v"(f[s][5]) : "v"(l16), "s"(hp) : "memory"); hp += 1024; wp += 5120; } while (0)
#define K2_GW(s, N) asm volatile("s_waitcnt vmcnt(" #N ")" : "+v"(f[s][0]), "+v"(f[s][1]), "+v"(f[s][2]), "+v"(f[s][3]), "+v"(f[s][4]), "+v"(f[s][5]) :: "memory")
#define K2_G1(s, u, LD) do { acc[u] = MFMA16(f[s][u], f[s][5], acc[u]); SBAR(); if (LD) K2_LW(s, u); if (GSLEEP) __builtin_amdgcn_s_sleep(GSLEEP); SBAR(); } while (0)
#define K2_GS(s, LD) do { K2_G1(s, 0, LD); K2_G1(s, 1, LD); K2_G1(s, 2, LD); K2_G1(s, 3, LD); K2_G1(s, 4, LD); if (LD) K2_LH(s); } while (0)
    RAW_BAR();
#pragma unroll
    for (int s = 0; s < 3; ++s) { K2_LW(s, 0); K2_LW(s, 1); K2_LW(s, 2); K2_LW(s, 3); K2_LW(s, 4); K2_LH(s); }
    STAMP(1);
    for (int k = 0; k < 8; ++k) {
      K2_GW(0, 12); K2_GS(0, true);
      K2_GW(1, 12); K2_GS(1, true);
      K2_GW(2, 12); K2_GS(2, true);
      if (k == 2) STAMP(2);
      if (k == 2 || k == 5) RAW_BAR();
    }
    K2_GW(0, 12); K2_GS(0, false);
    K2_GW(1, 6); K2_GS(1, false);
    K2_GW(2, 0); K2_GS(2, false);
    STAMP(3);
    u32x2 xv[5], bv[5];
#pragma unroll
    for (int u = 0; u < 5; ++u) {
      const int n = 160 * nb + 32 * u + 8 * i4 + 4 * hh;
      xv[u] = *(const u32x2*)(x + (32 * mt + r) * 1280 + n * 2); bv[u] = *(const u32x2*)(b2 + n * 2);
    }
    const lds_ptr red = lds + K2_RED + lane * 16;
    f32x4 own[5];
#pragma unroll
    for (int d = 0; d < 4; ++d) {
#pragma unroll
      for (int u = 0; u < 5; ++u) {
        const f32x4 q = {acc[u][4 * d], acc[u][4 * d + 1], acc[u][4 * d + 2], acc[u][4 * d + 3]};
        if (d == i4) own[u] = q; else stl<f32x4>(red + ((d * 3 + ((i4 - d - 1) & 3)) * 5 + u) * 1024, q);
      }
    }
    LGKM_BAR();
    char* pd = ws + WS_PART + (size_t)((mt * 4 + nb) * 4 + i4) * 5 * 1024 + lane * 16;
#pragma unroll
    for (int u = 0; u < 5; ++u) {
      f32x4 q = own[u];
#pragma unroll
      for (int s = 0; s < 3; ++s) q += ldl<f32x4>(red + ((i4 * 3 + s) * 5 + u) * 1024);
      q[0] += blo(xv[u][0]) + blo(bv[u][0]); q[1] += bhi(xv[u][0]) + bhi(bv[u][0]);
      q[2] += blo(xv[u][1]) + blo(bv[u][1]); q[3] += bhi(xv[u][1]) + bhi(bv[u][1]);
      *(f32x4*)(pd + u * 1024) = q;
    }
  }
  STAMP(4);
  PROF_END(1);
}

constexpr int K3_T = 40960, K3_LDS = K3_T + 2 * 32 * 656;

__global__ __launch_bounds__(640, 3) void k3(char* ws, char* out) {
  extern __shared__ __attribute__((aligned(16))) char smem[];
  const lds_ptr lds = (lds_ptr)smem;
  const unsigned lds0 = (unsigned)(uintptr_t)smem;
  const int lane = threadIdx.x & 63, w = __builtin_amdgcn_readfirstlane(threadIdx.x >> 6), r = lane & 31, hh = lane >> 5;
  const int bid = blockIdx.x, xcd = bid & 7, j = bid >> 3;
  const int mt = 16 * (xcd >> 1) + (j >> 1), nb = 2 * (xcd & 1) + (j & 1);
  const int u = w % 5, kh = w / 5;
  PROF_DECL STAMP(0);
#pragma unroll
  for (int i = 0; i < 4; ++i) glds16(ws + WS_OI + (size_t)(mt * 40 + w + 10 * i) * 1024 + lane * 16, lds0 + (w + 10 * i) * 1024);
  f32x16 acc = {};
  if (kh == 0) {
#pragma unroll
    for (int i = 0; i < 4; ++i) {
      const f32x4 q = *(const f32x4*)(ws + WS_PART + (size_t)(((mt * 4 + nb) * 4 + i) * 5 + u) * 1024 + lane * 16);
      acc[4 * i] = q[0]; acc[4 * i + 1] = q[1]; acc[4 * i + 2] = q[2]; acc[4 * i + 3] = q[3];
    }
  }
  bf16x8 wf[20];
  const char* wp = ws + WS_WOI + (size_t)((nb * 5 + u) * 40 + 20 * kh) * 1024 + lane * 16;
#pragma unroll
  for (int k = 0; k < 20; ++k) wf[k] = *(const bf16x8*)(wp + k * 1024);
  SBAR();
  STAMP(1);
  WAIT_BAR(20);
  STAMP(2);
  const lds_ptr op = lds + kh * 20480 + lane * 16;
#pragma unroll
  for (int k = 0; k < 20; ++k) acc = MFMA16(wf[k], ldl<bf16x8>(op + k * 1024), acc);
#pragma unroll
  for (int i = 0; i < 4; ++i)
    stl<f32x4>(lds + K3_T + (kh * 32 + r) * 656 + (32 * u + 8 * i + 4 * hh) * 4, f32x4{acc[4 * i], acc[4 * i + 1], acc[4 * i + 2], acc[4 * i + 3]});
  STAMP(3);
  LGKM_BAR();
  const int m = threadIdx.x / 20, c = threadIdx.x % 20;
  const lds_ptr tp = lds + K3_T + m * 656 + c * 32;
  const f32x4 a = ldl<f32x4>(tp) + ldl<f32x4>(tp + 32 * 656), b = ldl<f32x4>(tp + 16) + ldl<f32x4>(tp + 32 * 656 + 16);
  *(u32x4*)(out + (32 * mt + m) * 1280 + (160 * nb + 8 * c) * 2) = u32x4{cvtpk(a[0], a[1]), cvtpk(a[2], a[3]), cvtpk(b[0], b[1]), cvtpk(b[2], b[3])};
  STAMP(4);
  PROF_END(2);
}

extern "C" void kernel_launch(void* const* d_in, const int* in_sizes, int n_in, void* d_out, int out_size, void* d_ws, size_t ws_size,
                              hipStream_t stream) {
  const char* x = (const char*)d_in[0]; const char* wqkv = (const char*)d_in[1]; const char* wo = (const char*)d_in[2];
  const char* w1 = (const char*)d_in[4]; const char* b1 = (const char*)d_in[5]; const char* w2 = (const char*)d_in[6]; const char* b2 = (const char*)d_in[7];
  char* ws = (char*)d_ws;
  (void)hipFuncSetAttribute((const void*)k1, hipFuncAttributeMaxDynamicSharedMemorySize, K1_LDS);
  (void)hipFuncSetAttribute((const void*)k2, hipFuncAttributeMaxDynamicSharedMemorySize, K2_LDS);
  (void)hipFuncSetAttribute((const void*)k3, hipFuncAttributeMaxDynamicSharedMemorySize, K3_LDS);
  k1<<<256, 768, K1_LDS, stream>>>(x, wqkv, w1, b1, w2, wo, ws);
  k2<<<256, 768, K2_LDS, stream>>>(x, b2, ws);
  k3<<<256, 640, K3_LDS, stream>>>(ws, (char*)d_out);
  hipStreamCaptureStatus cs; (void)hipStreamIsCapturing(stream, &cs);
  static int calls = 0; ++calls;
  if (cs != hipStreamCaptureStatusNone) { fprintf(stderr, "DIAG call %d capturing\n", calls); return; }
  (void)hipStreamSynchronize(stream);
  std::vector<unsigned long long> t(4 * 256 * 16 * 8); (void)hipMemcpy(t.data(), ws + WS_PROF, t.size() * 8, hipMemcpyDeviceToHost);
  int nw[3] = {12, 12, 10}; unsigned long long first = 0;
  fprintf(stderr, "DIAG call %d:", calls);
  for (int k = 0; k < 3; ++k) { unsigned long long r0 = ~0ull, r1 = 0; for (int b = 0; b < 256; ++b) for (int w = 0; w < nw[k]; ++w) { auto* p = &t[((k * 256 + b) * 16 + w) * 8]; r0 = std::min(r0, p[6]); r1 = std::max(r1, p[7]); }
    if (!k) first = r0; fprintf(stderr, " k%d start %.2f span %.2f [", k + 1, (r0 - first) / 100.0, (r1 - r0) / 100.0);
    for (int w = 0; w < nw[k]; w += 4) { for (int i = 1; i <= 5; ++i) { std::vector<long long> v; for (int b = 0; b < 256; ++b) { auto* p = &t[((k * 256 + b) * 16 + w) * 8]; v.push_back((long long)(p[i] - p[0])); } std::sort(v.begin(), v.end()); fprintf(stderr, " %lld", v[128]); } fprintf(stderr, ";"); }
    fprintf(stderr, "]"); }
  fprintf(stderr, "\n");
}
```

```cpp
#include <hip/hip_runtime.h>
#include <hip/hip_bf16.h>
#include <stdint.h>
#pragma clang diagnostic ignored "-Winline-asm"

using bf16x8 = __attribute__((ext_vector_type(8))) short;
using s16x4 = __attribute__((ext_vector_type(4))) short;
using f32x16 = __attribute__((ext_vector_type(16))) float;
using f32x4 = __attribute__((ext_vector_type(4))) float;
using f32x2 = __attribute__((ext_vector_type(2))) float;
using u32x4 = __attribute__((ext_vector_type(4))) unsigned;
using u32x2 = __attribute__((ext_vector_type(2))) unsigned;
typedef __attribute__((address_space(3))) char* lds_ptr;

#define DEVI __device__ __forceinline__
#define SBAR() __builtin_amdgcn_sched_barrier(0)
#define PIN(x) asm volatile("" : "+v"(x))
#define MFMA16(a, b, c) __builtin_amdgcn_mfma_f32_32x32x16_bf16(a, b, c, 0, 0, 0)
#define MFMA8(a, b, c) __builtin_amdgcn_mfma_f32_32x32x8bf16_1k(a, b, c, 0, 0, 0)
#define MFMAS(a, b, c) __builtin_amdgcn_mfma_f32_16x16x32_bf16(a, b, c, 0, 0, 0)
#define WAIT_BAR(N) asm volatile("s_waitcnt vmcnt(" #N ") lgkmcnt(0)\n\ts_barrier" ::: "memory")
#define LGKM_BAR() asm volatile("s_waitcnt lgkmcnt(0)\n\ts_barrier" ::: "memory")
#define RAW_BAR() asm volatile("s_barrier" ::: "memory")

#define PROF_DECL
#define STAMP(i)
#define PROF_END(kid)

constexpr int SEQ = 2048, DM = 640, FF = 1728;
constexpr int QT = 1280, KVT = 2624;
constexpr size_t WS_QI = 0;
constexpr int KVH = 66 * KVT;
constexpr size_t WS_KVI = WS_QI + 32 * 64 * QT;
constexpr size_t WS_HI = WS_KVI + 32 * KVH;
constexpr size_t WS_W2I = WS_HI + 64 * 108 * 1024;
constexpr size_t WS_WOI = WS_W2I + 20 * 108 * 1024;
constexpr size_t WS_OI = WS_WOI + 20 * 40 * 1024;
constexpr size_t WS_PART = WS_OI + 64 * 40 * 1024;
constexpr float LOG2E = 1.4426950408889634f;
constexpr float QSCALE = 0.22360679774997896f * LOG2E;
constexpr float GC1 = -2.f * 0.7978845608028654f * LOG2E, GC2 = GC1 * 0.044715f;

__host__ __device__ constexpr int recpos(int t) { return t < 2 ? t : 2 + ((t - 2) & 7) + (((t - 2) & 24) << 1) + (((t - 2) & 32) >> 2); }
typedef __bf16 bf16x2_t __attribute__((ext_vector_type(2)));
DEVI unsigned cvtpk(float lo, float hi) { return __builtin_bit_cast(unsigned, __builtin_convertvector(f32x2{lo, hi}, bf16x2_t)); }
DEVI void glds16(const void* g, unsigned lds_base) {
  unsigned sv;
  asm volatile("s_mov_b32 %0, m0\n\ts_mov_b32 m0, %2\n\ts_nop 0\n\tglobal_load_lds_dwordx4 %1, off\n\ts_mov_b32 m0, %0" : "=&s"(sv) : "v"(g), "s"(lds_base) : "memory");
}
DEVI void glds16s(const void* sbase, unsigned voff, unsigned lds_base) {
  asm volatile("s_mov_b32 m0, %2\n\ts_nop 0\n\tglobal_load_lds_dwordx4 %0, %1" :: "v"(voff), "s"(sbase), "s"(lds_base) : "m0", "memory");
}
DEVI s16x4 ldtr(lds_ptr p) { return __builtin_amdgcn_ds_read_tr16_b64_v4i16((__attribute__((address_space(3))) s16x4*)p); }
DEVI bf16x8 ldtr2(lds_ptr p, int off2) { s16x4 a = ldtr(p), b = ldtr(p + off2); return __builtin_shufflevector(a, b, 0, 1, 2, 3, 4, 5, 6, 7); }
template <typename T> DEVI T ldl(lds_ptr p) { return *(__attribute__((address_space(3))) T*)p; }
template <typename T> DEVI void stl(lds_ptr p, T v) { *(__attribute__((address_space(3))) T*)p = v; }
DEVI float blo(unsigned d) { return __uint_as_float(d << 16); }
DEVI float bhi(unsigned d) { return __uint_as_float(d & 0xffff0000u); }
DEVI u32x4 pack8(const float* v) { return u32x4{cvtpk(v[0], v[1]), cvtpk(v[2], v[3]), cvtpk(v[4], v[5]), cvtpk(v[6], v[7])}; }
DEVI float gelu(float u) {
  float z = u * __builtin_fmaf(GC2, u * u, GC1);
  return u * __builtin_amdgcn_rcpf(1.f + __builtin_amdgcn_exp2f(z));
}

constexpr int K1_STG = 49152, K1_SP = 3 * K1_STG, K1_LDS = K1_SP + 16384;
constexpr int K1_RS = K1_SP, K1_CS = K1_SP + 1024;

struct K1Frag { bf16x8 x[2], w[2]; };

template <bool VF>
DEVI void k1_main(f32x16 (&acc)[2][2], f32x4 (&cs)[2], lds_ptr lds, int xa0, int wa0, bf16x8 sel) {
  lds_ptr xp[4], wp[2];
#pragma unroll
  for (int p = 0; p < 4; ++p) xp[p] = lds + (xa0 ^ (p * 32));
  wp[0] = lds + 16384 + wa0; wp[1] = lds + 16384 + (wa0 ^ 4096);
  K1Frag A, B;
  B.x[0] = B.x[1] = B.w[0] = B.w[1] = bf16x8{};
#define K1_LD(F, p) do { F.x[0] = ldl<bf16x8>(xp[p]); F.w[0] = ldtr2(wp[(p) & 1] + ((p) >> 1) * 8192, 1024); F.x[1] = ldl<bf16x8>(xp[p] + 4096); \
    F.w[1] = ldtr2(wp[(p) & 1] + ((p) >> 1) * 8192 + 16384, 1024); } while (0)
#define K1_MM(F) do { \
    if (VF) { acc[0][0] = MFMA16(F.x[0], F.w[0], acc[0][0]); acc[0][1] = MFMA16(F.x[1], F.w[0], acc[0][1]); } \
    else { acc[0][0] = MFMA16(F.w[0], F.x[0], acc[0][0]); acc[0][1] = MFMA16(F.w[0], F.x[1], acc[0][1]); } \
    acc[1][0] = MFMA16(F.w[1], F.x[0], acc[1][0]); acc[1][1] = MFMA16(F.w[1], F.x[1], acc[1][1]); } while (0)
#define K1_CSUM(F) do { cs[0] = MFMAS(F.w[0], sel, cs[0]); cs[1] = MFMAS(F.w[1], sel, cs[1]); } while (0)
  for (int s = 0; s < 10; ++s) {
    LGKM_BAR();
    K1_LD(A, 0); SBAR();
    K1_MM(B); SBAR();
    K1_LD(B, 1); SBAR();
    K1_MM(A); K1_CSUM(A); SBAR();
    K1_LD(A, 2); SBAR();
    K1_MM(B); SBAR();
    K1_LD(B, 3); SBAR();
    K1_MM(A); K1_CSUM(A); SBAR();
    const int dl = (s % 3 == 2) ? -2 * K1_STG : K1_STG;
#pragma unroll
    for (int p = 0; p < 4; ++p) xp[p] += dl;
    wp[0] += dl; wp[1] += dl;
  }
  K1_MM(B);
}

__global__ __launch_bounds__(768, 3) void k1(const char* __restrict__ x, const char* __restrict__ wqkv, const char* __restrict__ w1,
                                             const char* __restrict__ b1, const char* __restrict__ w2, const char* __restrict__ wo, char* __restrict__ ws) {
  extern __shared__ __attribute__((aligned(16))) char smem[];
  const lds_ptr lds = (lds_ptr)smem;
  const unsigned lds0 = (unsigned)(uintptr_t)smem;
  const int lane = threadIdx.x & 63, w = __builtin_amdgcn_readfirstlane(threadIdx.x >> 6), r = lane & 31, hh = lane >> 5;
  const int bid = blockIdx.x, x8 = bid & 7, j = bid >> 3;
  const int q4 = (lane & 15) >> 2, cb = 32 * ((lane >> 4) & 1) + 8 * (lane & 3);
  PROF_DECL STAMP(0);
  if (j >= 30) {
    const int hid = 2 * x8 + (j - 30), gw = 12 * hid + w;
#pragma unroll
    for (int rd = 0; rd < 2; ++rd) {
      char* dst[4]; int ust[4];
#pragma unroll
      for (int i = 0; i < 4; ++i) {
        int b = gw + 192 * (4 * rd + i); b = b < 1480 ? b : gw;
        const bool isw2 = b < 1080;
        const int bb = isw2 ? b : b - 1080, v = bb % 10, ks = bb / 10, slot = lane >> 3;
        const int row0 = isw2 ? 16 * ks + slot : (ks < 32 ? 20 * ks + slot : 20 * (4 * (ks - 32) + 2 * (slot >> 2)) + 16 + (slot & 3));
        const int rstep = (isw2 || ks < 32) ? 8 : 20;
        const char* src = (isw2 ? w2 : wo) + row0 * 1280 + v * 128 + (lane & 7) * 16;
        glds16(src, lds0 + w * 8192 + i * 2048); glds16(src + rstep * 1280, lds0 + w * 8192 + i * 2048 + 1024);
        const int u0 = 2 * v, u1 = 2 * v + 1;
        const int o0 = isw2 ? ((u0 / 5) * 108 + ks) * 5120 + (u0 % 5) * 1024 : (u0 * 40 + ks) * 1024;
        const int o1 = isw2 ? ((u1 / 5) * 108 + ks) * 5120 + (u1 % 5) * 1024 : (u1 * 40 + ks) * 1024;
        ust[i] = o1 - o0;
        dst[i] = ws + (isw2 ? WS_W2I : WS_WOI) + o0 + lane * 16;
      }
      if (rd == 0) {
        const int t = hid * 768 + threadIdx.x;
        const unsigned cv = (t & 1) ? 0u : 0x3f803f80u;
        if (t < 8448) *(u32x4*)(ws + WS_KVI + (size_t)(t >> 2) * KVT + 1280 + 640 + ((t >> 1) & 1) * 672 + (t & 1) * 16) = u32x4{cv, cv, cv, cv};
      }
      asm volatile("s_waitcnt vmcnt(0)" ::: "memory");
      const lds_ptr rp = lds + w * 8192 + (4 * hh + q4) * 128 + cb;
#pragma unroll
      for (int i = 0; i < 4; ++i) {
        const bf16x8 a = ldtr2(rp + i * 2048, 1024), b = ldtr2(rp + i * 2048 + 64, 1024);
        *(bf16x8*)dst[i] = a; *(bf16x8*)(dst[i] + ust[i]) = b;
      }
      asm volatile("s_waitcnt lgkmcnt(0)" ::: "memory");
    }
    PROF_END(0);
    return;
  }
  const int L = 30 * x8 + j, mhalf = L >= 120, Lr = L - 120 * mhalf, n = Lr >> 3, mg = 8 * mhalf + (Lr & 7);
  if (w >= 8) {
    const int lw = w - 8;
    unsigned xo[4], qo[4], mo[4];
    const int ch = (lane & 15) ^ ((lane >> 4) << 2), chm = n >= 13 ? (ch & 7) : ch;
#pragma unroll
    for (int i = 0; i < 4; ++i) {
      xo[i] = (32 * lw + 8 * i + (lane >> 3)) * 1280 + (((lane & 7) ^ ((4 * i + (lane >> 4)) & 7)) * 16);
      qo[i] = (16 * lw + 4 * i + (lane >> 4)) * 3840 + ch * 16;
      mo[i] = (16 * lw + 4 * i + (lane >> 4)) * 3456 + chm * 16;
    }
    const char* xb = x + mg * (128 * 1280);
    const char* qb = wqkv + n * 256;
    const char* mb = w1 + (n < 13 ? n : 13) * 256;
    unsigned nb = lds0 + lw * 4096;
#pragma unroll
    for (int s = 0; s < 2; ++s) {
#pragma unroll
      for (int i = 0; i < 4; ++i) { glds16s(xb, xo[i], nb + i * 1024); glds16s(qb, qo[i], nb + 16384 + i * 1024); glds16s(mb, mo[i], nb + 32768 + i * 1024); }
      xb += 128; qb += 64 * 3840; mb += 64 * 3456; nb += K1_STG;
    }
    lds_ptr sp[4];
#pragma unroll
    for (int i = 0; i < 4; ++i) sp[i] = lds + (32 * lw + r) * 128 + (((2 * i + hh) ^ ((r >> 1) & 7)) * 16);
    float s0 = 0.f, s1 = 0.f, q0 = 0.f, q1 = 0.f;
    STAMP(1);
    for (int s = 0; s < 10; ++s) {
      if (s < 9) WAIT_BAR(12); else WAIT_BAR(0);
      if (s == 0) STAMP(2);
      u32x4 d[4];
#pragma unroll
      for (int i = 0; i < 4; ++i) d[i] = ldl<u32x4>(sp[i]);
      SBAR();
#define K1_ST(v) do { const float lo = blo(v), hi = bhi(v); \
        asm("v_add_f32 %0, %0, %2\n\tv_fmac_f32 %1, %2, %2" : "+v"(s0), "+v"(q0) : "v"(lo)); asm("v_add_f32 %0, %0, %2\n\tv_fmac_f32 %1, %2, %2" : "+v"(s1), "+v"(q1) : "v"(hi)); } while (0)
      if (s < 8) {
#pragma unroll
        for (int i = 0; i < 4; ++i) {
          glds16s(xb, xo[i], nb + i * 1024); K1_ST(d[i][0]); SBAR();
          glds16s(qb, qo[i], nb + 16384 + i * 1024); K1_ST(d[i][1]); SBAR();
          glds16s(mb, mo[i], nb + 32768 + i * 1024); K1_ST(d[i][2]); K1_ST(d[i][3]); SBAR();
        }
        xb += 128; qb += 64 * 3840; mb += 64 * 3456;
        nb = (s % 3 == 0) ? nb - 2 * K1_STG : nb + K1_STG;
      } else {
#pragma unroll
        for (int i = 0; i < 4; ++i) { K1_ST(d[i][0]); K1_ST(d[i][1]); K1_ST(d[i][2]); K1_ST(d[i][3]); }
      }
      const int dl = (s % 3 == 2) ? -2 * K1_STG : K1_STG;
#pragma unroll
      for (int i = 0; i < 4; ++i) sp[i] += dl;
    }
    STAMP(3);
    s0 += s1; q0 += q1;
    auto a = __builtin_amdgcn_permlane32_swap(__float_as_uint(s0), __float_as_uint(q0), false, false);
    const float t = __uint_as_float(a[0]) + __uint_as_float(a[1]);
    auto b = __builtin_amdgcn_permlane32_swap(__float_as_uint(t), __float_as_uint(t), false, false);
    const float mean = __uint_as_float(b[0]) * (1.f / 640.f);
    const float var = __builtin_fmaxf(__builtin_fmaf(-mean, mean, __uint_as_float(b[1]) * (1.f / 640.f)), 0.f);
    const float rstd = __builtin_amdgcn_rsqf(var + 1e-5f);
    stl<float>(lds + K1_RS + hh * 512 + (32 * lw + r) * 4, hh ? -mean * rstd : rstd);
    LGKM_BAR();
    STAMP(4);
    PROF_END(0);
    return;
  }
  const int mh = w & 1, np = w >> 1;
  const int fb = 128 * n + 32 * np;
  const bool mlp_ok = fb < FF;
  u32x2 bias[4];
#pragma unroll
  for (int i = 0; i < 4; ++i) bias[i] = *(const u32x2*)(b1 + (mlp_ok ? fb + 8 * i + 4 * hh : 0) * 2);
  const int xa0 = (64 * mh + r) * 128 + (((2 * mh + hh) ^ ((r >> 1) & 7)) * 16);
  const int wa0 = (16 * mh + 8 * hh + q4) * 256 + ((64 * np + cb) ^ (q4 << 6));
  const short one = ((lane & 15) == ((lane >> 4) & 1)) ? (short)0x3f80 : (short)0;
  const bf16x8 sel = {one, one, one, one, one, one, one, one};
  f32x16 acc[2][2] = {};
  f32x4 cs[2] = {};
  STAMP(1);
  if (n < 10) k1_main<false>(acc, cs, lds, xa0, wa0, sel);
  else k1_main<true>(acc, cs, lds, xa0, wa0, sel);
  STAMP(2);
  PIN(bias[0]); PIN(bias[1]); PIN(bias[2]); PIN(bias[3]);
  const lds_ptr CS = lds + K1_CS + np * 256;
  if ((lane & 15) < 2) {
    stl<f32x4>(CS + mh * 1024 + (16 * (lane & 1) + 4 * (lane >> 4)) * 4, cs[0]);
    stl<f32x4>(CS + mh * 1024 + 128 + (16 * (lane & 1) + 4 * (lane >> 4)) * 4, cs[1]);
  }
  LGKM_BAR();
  const int mt0 = 4 * mg + 2 * mh;
  const lds_ptr RS = lds + K1_RS + 256 * mh;
  float rs[2], nm[2];
#pragma unroll
  for (int t = 0; t < 2; ++t) { rs[t] = ldl<float>(RS + (32 * t + r) * 4); nm[t] = ldl<float>(RS + 512 + (32 * t + r) * 4); }
  if (n < 10) {
    const bool isq = n < 5;
    const float sc = isq ? QSCALE : 1.f;
    const int tb = isq ? QT : KVT, hb = isq ? 64 * QT : KVH;
    char* base = ws + (isq ? WS_QI + mt0 * QT : WS_KVI + recpos(mt0) * KVT);
    const int q0 = 32 * (isq ? n : n - 5) + 8 * np + hh;
#pragma unroll
    for (int g = 0; g < 4; ++g) {
      const f32x4 c = ldl<f32x4>(CS + (8 * g + 4 * hh) * 4) + ldl<f32x4>(CS + 1024 + (8 * g + 4 * hh) * 4);
      const int qd = q0 + 2 * g, hd = (qd * 205) >> 10, dq = qd - 5 * hd;
      char* dst = base + hd * hb + (dq < 4 ? (dq & 1) * 512 + r * 16 + (dq >> 1) * 8 : 1024 + r * 8);
#pragma unroll
      for (int t = 0; t < 2; ++t) {
        const float a = rs[t] * sc, b = nm[t] * sc;
        float v[4];
#pragma unroll
        for (int e = 0; e < 4; ++e) v[e] = __builtin_fmaf(a, acc[0][t][4 * g + e], b * c[e]);
        *(u32x2*)(dst + t * tb) = u32x2{cvtpk(v[0], v[1]), cvtpk(v[2], v[3])};
      }
    }
  } else {
    const float c = ldl<float>(CS + r * 4) + ldl<float>(CS + 1024 + r * 4);
    const int col = 128 * (n - 10) + 32 * np + r, hd = (col * 3277) >> 16, d = col - 20 * hd;
    char* dst = ws + WS_KVI + hd * KVH + 1280 + hh * 320 + d * 16;
    const int ro[3] = {recpos(mt0 + 1) * KVT, recpos(mt0 + 2) * KVT, recpos(mt0 + 2) * KVT + KVT};
#pragma unroll
    for (int t = 0; t < 2; ++t) {
      float v[16];
#pragma unroll
      for (int i = 0; i < 4; ++i) {
        const f32x4 a = ldl<f32x4>(RS + (32 * t + 8 * i + 4 * hh) * 4), b = ldl<f32x4>(RS + 512 + (32 * t + 8 * i + 4 * hh) * 4);
#pragma unroll
        for (int e = 0; e < 4; ++e) v[4 * i + e] = __builtin_fmaf(a[e], acc[0][t][4 * i + e], b[e] * c);
      }
      *(u32x4*)(dst + ro[t]) = pack8(v); *(u32x4*)(dst + ro[t + 1] + 672) = pack8(v + 8);
    }
  }
  STAMP(3);
  if (mlp_ok) {
    f32x4 c4[4];
#pragma unroll
    for (int i = 0; i < 4; ++i) c4[i] = ldl<f32x4>(CS + 128 + (8 * i + 4 * hh) * 4) + ldl<f32x4>(CS + 1024 + 128 + (8 * i + 4 * hh) * 4);
    char* hdst = ws + WS_HI + (size_t)(mt0 * 108 + 8 * n + 2 * np) * 1024 + lane * 16;
#pragma unroll
    for (int t = 0; t < 2; ++t) {
      float v[16];
#pragma unroll
      for (int i = 0; i < 16; ++i) {
        const unsigned bd = bias[i >> 2][(i >> 1) & 1];
        v[i] = gelu(__builtin_fmaf(rs[t], acc[1][t][i], __builtin_fmaf(nm[t], c4[i >> 2][i & 3], (i & 1) ? bhi(bd) : blo(bd))));
      }
      *(u32x4*)(hdst + t * 108 * 1024) = pack8(v);
      *(u32x4*)(hdst + t * 108 * 1024 + 1024) = pack8(v + 8);
    }
  }
  STAMP(4);
  PROF_END(0);
}

constexpr int K2_CH = 16 * KVT, K2_XCH = K2_CH, K2_RED = K2_CH + 24576, K2_LDS = K2_RED + 81920;

DEVI float max16(const f32x16& s) {
  float a = __builtin_fmaxf(__builtin_fmaxf(s[0], s[1]), s[2]), b = __builtin_fmaxf(__builtin_fmaxf(s[3], s[4]), s[5]);
  a = __builtin_fmaxf(__builtin_fmaxf(a, s[6]), s[7]); b = __builtin_fmaxf(__builtin_fmaxf(b, s[8]), s[9]);
  a = __builtin_fmaxf(__builtin_fmaxf(a, s[10]), s[11]); b = __builtin_fmaxf(__builtin_fmaxf(b, s[12]), s[13]);
  a = __builtin_fmaxf(__builtin_fmaxf(a, s[14]), s[15]); a = __builtin_fmaxf(a, b);
  auto t = __builtin_amdgcn_permlane32_swap(__float_as_uint(a), __float_as_uint(a), false, false);
  return __builtin_fmaxf(__uint_as_float(t[0]), __uint_as_float(t[1]));
}

#define VZ 656
struct K2S {
  f32x16 s[2][2], o[2], acc[3];
  u32x4 p[2][2];
  bf16x8 qm[2], km, v0, v1, f[2][4];
  s16x4 qr[2], kr;
  lds_ptr kmp, krp, vp;
  const char *hp, *wp, *src;
  unsigned dst, l16, woff;
  unsigned long long msk;
};
#define BC(p) __builtin_bit_cast(bf16x8, p)
enum { Q8A, Q8B, P1A, P1B, Q16A, Q16B, P0A, P0B, G0, G1, G2, FL };
__host__ __device__ constexpr int k2_slot(int ng, int op) {
  constexpr int pos0[8] = {0, 6, 12, 18, 24, 30, 36, 42}, id0[8] = {Q8A, Q8B, P1A, P1B, Q16A, Q16B, P0A, P0B};
  constexpr int pos2[10] = {0, 4, 9, 14, 19, 24, 28, 33, 38, 43}, id2[10] = {Q8A, Q8B, G0, P1A, P1B, Q16A, Q16B, G1, P0A, P0B};
  constexpr int pos3[11] = {0, 4, 9, 13, 17, 22, 26, 30, 35, 39, 43}, id3[11] = {Q8A, Q8B, G0, P1A, P1B, G1, Q16A, Q16B, G2, P0A, P0B};
  if (op == 41) return FL;
  for (int i = 0; i < 8 + ng; ++i) if ((ng == 0 ? pos0[i] : ng == 2 ? pos2[i] : pos3[i]) == op) return ng == 0 ? id0[i] : ng == 2 ? id2[i] : id3[i];
  return -1;
}
template <int J> DEVI void k2_fill(K2S& z) {
  if (J < 5) glds16s(z.src + J * 8192, z.l16, z.dst + J * 8192);
  else asm volatile("s_mov_b32 m0, %2\n\ts_mov_b64 exec, %3\n\tglobal_load_lds_dwordx4 %0, %1\n\ts_mov_b64 exec, -1"
                    :: "v"(z.l16), "s"(z.src - z.woff + 40960), "s"(z.dst - z.woff + 40960), "s"(z.msk) : "m0", "memory");
}
template <int NU, int S> DEVI void k2_ldw(K2S& z, int u) {
  if (u == 0) asm volatile("global_load_dwordx4 %0, %1, %2" : "=v"(z.f[S][0]) : "v"(z.l16), "s"(z.wp) : "memory");
  if (u == 1) asm volatile("global_load_dwordx4 %0, %1, %2 offset:1024" : "=v"(z.f[S][1]) : "v"(z.l16), "s"(z.wp) : "memory");
  if (u == 2) asm volatile("global_load_dwordx4 %0, %1, %2 offset:2048" : "=v"(z.f[S][2]) : "v"(z.l16), "s"(z.wp) : "memory");
  if (u == NU - 1) { asm volatile("global_load_dwordx4 %0, %1, %2" : "=v"(z.f[S][3]) : "v"(z.l16), "s"(z.hp) : "memory"); z.hp += 1024; z.wp += 5120; }
}
template <int NU, int I, int GM, bool FILL, int VM, int OP = 0> DEVI void k2_tile(K2S& z) {
  constexpr int P = I & 1, m = k2_slot(GM ? NU : 0, OP), off = I * KVT;
  f32x16 (&S)[2] = z.s[P], (&N)[2] = z.s[P ^ 1];
  if constexpr (m == Q8A) N[0] = MFMA8(z.kr, z.qr[0], f32x16{});
  if constexpr (m == Q8B) { N[1] = MFMA8(z.kr, z.qr[1], f32x16{}); SBAR(); z.kr = ldl<s16x4>(z.krp + off); }
  if constexpr (m == P1A) z.o[0] = MFMA16(z.v1, BC(z.p[1][0]), z.o[0]);
  if constexpr (m == P1B) { z.o[1] = MFMA16(z.v1, BC(z.p[1][1]), z.o[1]); SBAR(); z.v1 = ldl<bf16x8>(z.vp + off + 672); }
  if constexpr (m == Q16A) N[0] = MFMA16(z.km, z.qm[0], N[0]);
  if constexpr (m == Q16B) { N[1] = MFMA16(z.km, z.qm[1], N[1]); SBAR(); z.km = ldl<bf16x8>(z.kmp + off); }
  if constexpr (m == P0A) z.o[0] = MFMA16(z.v0, BC(z.p[0][0]), z.o[0]);
  if constexpr (m == P0B) { z.o[1] = MFMA16(z.v0, BC(z.p[0][1]), z.o[1]); SBAR(); z.v0 = ldl<bf16x8>(z.vp + off); }
  if constexpr (m == G0) {
    if constexpr (NU == 3) asm volatile("s_waitcnt vmcnt(%4)" : "+v"(z.f[P][0]), "+v"(z.f[P][1]), "+v"(z.f[P][2]), "+v"(z.f[P][3]) : "n"(VM) : "memory");
    else asm volatile("s_waitcnt vmcnt(%3)" : "+v"(z.f[P][0]), "+v"(z.f[P][1]), "+v"(z.f[P][3]) : "n"(VM) : "memory");
  }
  if constexpr (m >= G0 && m <= G2) {
    z.acc[m - G0] = MFMA16(z.f[P][m - G0], z.f[P][3], z.acc[m - G0]); SBAR();
    if (GM == 2) k2_ldw<NU, P>(z, m - G0);
  }
  if constexpr (m == FL && FILL && I < 6) k2_fill<I>(z);
  SBAR();
  constexpr int pr = OP / 3, k = OP % 3;
  if constexpr (k < 2) { constexpr int e = (pr >> 3) * 8 + (pr & 3) * 2 + k; S[(pr >> 2) & 1][e] = __builtin_amdgcn_exp2f(S[(pr >> 2) & 1][e]); }
  else if constexpr (pr == 0) z.p[1][1][3] = cvtpk(N[1][14], N[1][15]);
  else { constexpr int q = pr - 1, e = (q >> 3) * 8 + (q & 3) * 2; z.p[q >> 3][(q >> 2) & 1][q & 3] = cvtpk(S[(q >> 2) & 1][e], S[(q >> 2) & 1][e + 1]); }
  SBAR();
  if constexpr (OP < 47) k2_tile<NU, I, GM, FILL, VM, OP + 1>(z);
}
#define APRIO(x)
template <int NU> DEVI void k2_loop(K2S& z) {
  constexpr int L = NU + 1;
  k2_ldw<NU, 0>(z, 0); k2_ldw<NU, 0>(z, 1); if (NU == 3) k2_ldw<NU, 0>(z, 2);
  k2_ldw<NU, 1>(z, 0); k2_ldw<NU, 1>(z, 1); if (NU == 3) k2_ldw<NU, 1>(z, 2);
  for (int c = 0; c < 3; ++c) {
    asm volatile("s_waitcnt vmcnt(%0) lgkmcnt(0)\n\ts_barrier" :: "n"(2 * L) : "memory");
    z.src += K2_CH;
    z.dst = (c & 1) ? z.dst + K2_CH : z.dst - K2_CH;
    APRIO(3);
    k2_tile<NU, 0, 2, true, L>(z); k2_tile<NU, 1, 2, true, L + 1>(z); k2_tile<NU, 2, 2, true, L + 2>(z);
    APRIO(2);
    k2_tile<NU, 3, 2, true, L + 2>(z); k2_tile<NU, 4, 2, true, L + 2>(z); k2_tile<NU, 5, 2, true, L + 2>(z);
    APRIO(1);
    k2_tile<NU, 6, 2, true, L + 2>(z);
    APRIO(0);
    k2_tile<NU, 7, 2, true, L + 1>(z);
    const int dl = (c & 1) ? K2_CH : -K2_CH;
    z.kmp += dl; z.krp += dl; z.vp += dl;
  }
  asm volatile("s_waitcnt vmcnt(%0) lgkmcnt(0)\n\ts_barrier" :: "n"(2 * L) : "memory");
  APRIO(3);
  k2_tile<NU, 0, 2, false, L>(z); k2_tile<NU, 1, 1, false, L>(z); k2_tile<NU, 2, 1, false, 0>(z);
  APRIO(2);
  k2_tile<NU, 3, 0, false, 0>(z); k2_tile<NU, 4, 0, false, 0>(z); k2_tile<NU, 5, 0, false, 0>(z);
  APRIO(1);
  k2_tile<NU, 6, 0, false, 0>(z);
  APRIO(0);
  k2_tile<NU, 7, 0, false, 0>(z);
  z.p[1][1][3] = cvtpk(z.s[1][1][14], z.s[1][1][15]);
  z.o[0] = MFMA16(z.v1, BC(z.p[1][0]), z.o[0]); z.o[1] = MFMA16(z.v1, BC(z.p[1][1]), z.o[1]);
}

template <int kh> DEVI void k2_body(const char* __restrict__ x, const char* __restrict__ b2, char* __restrict__ ws, const int w) {
  extern __shared__ __attribute__((aligned(16))) char smem[];
  const lds_ptr lds = (lds_ptr)smem;
  const unsigned lds0 = (unsigned)(uintptr_t)smem;
  const int lane = threadIdx.x & 63, r = lane & 31, hh = lane >> 5;
  const int bid = blockIdx.x, xcd = bid & 7;
  const unsigned l16 = lane * 16;
  PROF_DECL STAMP(0);
  const int h = 4 * xcd + ((bid >> 3) & 3), kq = w & 3, mt = 8 * (bid >> 5) + 2 * kq;
  const int gmt = 8 * (bid >> 5) + 2 * (xcd >> 1) + ((bid >> 4) & 1), nb = 2 * (xcd & 1) + ((bid >> 3) & 1);
  const char* kv = ws + WS_KVI + (size_t)h * KVH;
  const char* qp = ws + WS_QI + (size_t)(h * 64 + mt) * QT;
  const char* k0p = kv + (kh ? recpos(32) * KVT : 0);
  const unsigned r8 = 1024 + r * 8, vo = 1280 + (r < 20 ? hh * 320 + r * 16 : r == 20 ? 640 : VZ);
  K2S z;
  bf16x8 kmf, km0; s16x4 krf, kr0;
  z.l16 = l16; z.woff = w * 1024; z.src = kv + 2 * KVT + z.woff; z.dst = lds0 + K2_CH + z.woff; z.msk = 0xffull << (8 * w);
  z.hp = ws + WS_HI + (size_t)(gmt * 108 + 27 * kq) * 1024;
  z.wp = ws + WS_W2I + (size_t)(nb * 108 + 27 * kq) * 5120 + kh * 3072;
  asm volatile("global_load_dwordx4 %0, %11, %13\n\tglobal_load_dwordx2 %1, %12, %13\n\tglobal_load_dwordx4 %2, %11, %14\n\tglobal_load_dwordx2 %3, %12, %14\n\t"
               "global_load_dwordx4 %4, %11, %13 offset:1280\n\tglobal_load_dwordx2 %5, %12, %13 offset:1280\n\t"
               "global_load_dwordx4 %6, %11, %15\n\tglobal_load_dwordx2 %7, %12, %15\n\t"
               "global_load_dwordx2 %8, %12, %15 offset:2624\n\tglobal_load_dwordx4 %9, %11, %15 offset:2624\n\tglobal_load_dwordx4 %10, %16, %15 offset:2624"
               : "=&v"(z.qm[0]), "=&v"(z.qr[0]), "=&v"(kmf), "=&v"(krf), "=&v"(z.qm[1]), "=&v"(z.qr[1]), "=&v"(km0), "=&v"(kr0), "=&v"(z.kr), "=&v"(z.km), "=&v"(z.v0)
               : "v"(l16), "v"(r8), "s"(qp), "s"(kv), "s"(k0p), "v"(vo) : "memory");
  k2_fill<0>(z); k2_fill<1>(z); k2_fill<2>(z); k2_fill<3>(z); k2_fill<4>(z); k2_fill<5>(z);
  asm volatile("s_waitcnt vmcnt(11)" : "+v"(z.qm[0]), "+v"(z.qr[0]), "+v"(kmf), "+v"(krf), "+v"(z.qm[1]), "+v"(z.qr[1]) :: "memory");
  if (hh) { z.qr[0] = s16x4{}; z.qr[1] = s16x4{}; }
  {
    f32x16 sA = MFMA8(krf, z.qr[0], f32x16{}), sB = MFMA8(krf, z.qr[1], f32x16{});
    sA = MFMA16(kmf, z.qm[0], sA); sB = MFMA16(kmf, z.qm[1], sB);
    const unsigned mA = cvtpk(-max16(sA), 0.f), mB = cvtpk(-max16(sB), 0.f);
    if (hh) { z.qr[0][0] = (short)mA; z.qr[1][0] = (short)mB; }
  }
  SBAR();
  asm volatile("s_waitcnt vmcnt(6)" : "+v"(km0), "+v"(kr0), "+v"(z.kr), "+v"(z.km), "+v"(z.v0) :: "memory");
  if (hh) { kr0 = s16x4{0x3f80, 0x3f80, 0x3f80, 0x3f80}; z.kr = kr0; }
  z.s[0][0] = MFMA8(kr0, z.qr[0], f32x16{}); z.s[0][1] = MFMA8(kr0, z.qr[1], f32x16{});
  z.s[0][0] = MFMA16(km0, z.qm[0], z.s[0][0]); z.s[0][1] = MFMA16(km0, z.qm[1], z.s[0][1]);
  z.s[1][0] = z.s[1][1] = z.o[0] = z.o[1] = z.acc[0] = z.acc[1] = z.acc[2] = f32x16{};
  z.v1 = bf16x8{};
  z.p[1][0] = z.p[1][1] = u32x4{};
  z.kmp = lds + K2_CH + kh * 8 * KVT + l16; z.krp = lds + K2_CH + kh * 8 * KVT + (hh ? 1920 : r8); z.vp = lds + K2_CH + kh * 8 * KVT + vo;
  SBAR();
  STAMP(1);
  k2_loop<3 - kh>(z);
  STAMP(3);
  constexpr int nu = 3 - kh;
  u32x2 xv[3], bv[3];
#pragma unroll
  for (int u = 0; u < nu; ++u) {
    const int n = 160 * nb + 32 * (3 * kh + u) + 8 * kq + 4 * hh;
    xv[u] = *(const u32x2*)(x + (32 * gmt + r) * 1280 + n * 2); bv[u] = *(const u32x2*)(b2 + n * 2);
  }
  f32x16 o = kh ? z.o[1] : z.o[0];
  {
    const f32x16 g = kh ? z.o[0] : z.o[1];
    const lds_ptr xw = lds + K2_XCH + w * 3072 + l16, xr = lds + K2_XCH + (w ^ 4) * 3072 + l16;
#pragma unroll
    for (int i = 0; i < 3; ++i) stl<f32x4>(xw + i * 1024, f32x4{g[4 * i], g[4 * i + 1], g[4 * i + 2], g[4 * i + 3]});
    const lds_ptr rw = lds + K2_RED + (kq * 5 + 3 * kh) * 1024 + l16;
#pragma unroll
    for (int u = 0; u < 3; ++u) {
      if (u < nu) {
#pragma unroll
        for (int d = 0; d < 4; ++d) stl<f32x4>(rw + (d * 20 + u) * 1024, f32x4{z.acc[u][4 * d], z.acc[u][4 * d + 1], z.acc[u][4 * d + 2], z.acc[u][4 * d + 3]});
      }
    }
    LGKM_BAR();
#pragma unroll
    for (int i = 0; i < 3; ++i) { const f32x4 q = ldl<f32x4>(xr + i * 1024); o[4 * i] += q[0]; o[4 * i + 1] += q[1]; o[4 * i + 2] += q[2]; o[4 * i + 3] += q[3]; }
  }
  auto lt = __builtin_amdgcn_permlane32_swap(__float_as_uint(o[8]), __float_as_uint(o[8]), false, false);
  float l = __uint_as_float(lt[1]);
  float om = 0.f;
#pragma unroll
  for (int i = 0; i < 12; ++i) om = __builtin_fmaxf(om, __builtin_fabsf(o[i]));
  const bool bad = !(l >= 0x1p-30f && l <= 0x1p60f && om <= 0x1p126f);
  if (__builtin_amdgcn_ballot_w64(bad) != 0) {
    const bf16x8 qm = kh ? z.qm[1] : z.qm[0];
    s16x4 qr = kh ? z.qr[1] : z.qr[0];
    if (hh) qr = s16x4{};
    float m = -__builtin_inff();
    o = f32x16{};
    for (int t = 0; t < 64; ++t) {
      const char* tp = kv + recpos(t) * KVT;
      const bf16x8 a = *(const bf16x8*)(tp + l16), va = *(const bf16x8*)(kv + recpos(t + 1) * KVT + vo), vb = *(const bf16x8*)(kv + recpos(t + 2) * KVT + vo + 672);
      const s16x4 b = *(const s16x4*)(tp + r8);
      f32x16 s = MFMA8(b, qr, f32x16{}); s = MFMA16(a, qm, s);
      const float mn = __builtin_fmaxf(m, max16(s)), al = __builtin_amdgcn_exp2f(m - mn);
      m = mn;
      float p[16];
#pragma unroll
      for (int i = 0; i < 16; ++i) { p[i] = __builtin_amdgcn_exp2f(s[i] - mn); o[i] *= al; }
      o = MFMA16(va, BC(pack8(p)), o);
      o = MFMA16(vb, BC(pack8(p + 8)), o);
    }
    lt = __builtin_amdgcn_permlane32_swap(__float_as_uint(o[8]), __float_as_uint(o[8]), false, false);
    l = __uint_as_float(lt[1]);
  }
  const float il = __builtin_amdgcn_rcpf(l);
  float v[12];
#pragma unroll
  for (int i = 0; i < 12; ++i) v[i] = o[i] * il;
  char* od = ws + WS_OI + (size_t)(mt + kh) * 40 * 1024;
  *(u32x4*)(od + h * 1024 + lane * 16) = pack8(v);
  if (!hh) *(u32x2*)(od + (32 + (h >> 2)) * 1024 + ((h >> 1) & 1) * 512 + r * 16 + (h & 1) * 8) = u32x2{cvtpk(v[8], v[9]), cvtpk(v[10], v[11])};
  STAMP(2);
  char* pd = ws + WS_PART + (size_t)(((gmt * 4 + nb) * 4 + kq) * 5 + 3 * kh) * 1024 + lane * 16;
  const lds_ptr rr = lds + K2_RED + (kq * 20 + 3 * kh) * 1024 + l16;
#pragma unroll
  for (int u = 0; u < 3; ++u) {
    if (u < nu) {
      f32x4 q = ldl<f32x4>(rr + u * 1024);
#pragma unroll
      for (int s = 1; s < 4; ++s) q += ldl<f32x4>(rr + (s * 5 + u) * 1024);
      q[0] += blo(xv[u][0]) + blo(bv[u][0]); q[1] += bhi(xv[u][0]) + bhi(bv[u][0]);
      q[2] += blo(xv[u][1]) + blo(bv[u][1]); q[3] += bhi(xv[u][1]) + bhi(bv[u][1]);
      *(f32x4*)(pd + u * 1024) = q;
    }
  }
  STAMP(4);
  PROF_END(1);
}
__global__ __launch_bounds__(512, 2) void k2(const char* __restrict__ x, const char* __restrict__ b2, char* __restrict__ ws) {
  const int w = __builtin_amdgcn_readfirstlane(threadIdx.x >> 6);
  if (w < 4) k2_body<0>(x, b2, ws, w); else k2_body<1>(x, b2, ws, w);
}

constexpr int K3_T = 40960, K3_LDS = K3_T + 2 * 32 * 656;

__global__ __launch_bounds__(640, 3) void k3(char* ws, char* out) {
  extern __shared__ __attribute__((aligned(16))) char smem[];
  const lds_ptr lds = (lds_ptr)smem;
  const unsigned lds0 = (unsigned)(uintptr_t)smem;
  const int lane = threadIdx.x & 63, w = __builtin_amdgcn_readfirstlane(threadIdx.x >> 6), r = lane & 31, hh = lane >> 5;
  const int bid = blockIdx.x, xcd = bid & 7, j = bid >> 3;
  const int mt = 16 * (xcd >> 1) + (j >> 1), nb = 2 * (xcd & 1) + (j & 1);
  const int u = w % 5, kh = w / 5;
  PROF_DECL STAMP(0);
#pragma unroll
  for (int i = 0; i < 4; ++i) glds16(ws + WS_OI + (size_t)(mt * 40 + w + 10 * i) * 1024 + lane * 16, lds0 + (w + 10 * i) * 1024);
  f32x16 acc = {};
  if (kh == 0) {
#pragma unroll
    for (int i = 0; i < 4; ++i) {
      const f32x4 q = *(const f32x4*)(ws + WS_PART + (size_t)(((mt * 4 + nb) * 4 + i) * 5 + u) * 1024 + lane * 16);
      acc[4 * i] = q[0]; acc[4 * i + 1] = q[1]; acc[4 * i + 2] = q[2]; acc[4 * i + 3] = q[3];
    }
  }
  bf16x8 wf[20];
  const char* wp = ws + WS_WOI + (size_t)((nb * 5 + u) * 40 + 20 * kh) * 1024 + lane * 16;
#pragma unroll
  for (int k = 0; k < 20; ++k) wf[k] = *(const bf16x8*)(wp + k * 1024);
  SBAR();
  STAMP(1);
  WAIT_BAR(20);
  STAMP(2);
  const lds_ptr op = lds + kh * 20480 + lane * 16;
#pragma unroll
  for (int k = 0; k < 20; ++k) acc = MFMA16(wf[k], ldl<bf16x8>(op + k * 1024), acc);
#pragma unroll
  for (int i = 0; i < 4; ++i)
    stl<f32x4>(lds + K3_T + (kh * 32 + r) * 656 + (32 * u + 8 * i + 4 * hh) * 4, f32x4{acc[4 * i], acc[4 * i + 1], acc[4 * i + 2], acc[4 * i + 3]});
  STAMP(3);
  LGKM_BAR();
  const int m = threadIdx.x / 20, c = threadIdx.x % 20;
  const lds_ptr tp = lds + K3_T + m * 656 + c * 32;
  const f32x4 a = ldl<f32x4>(tp) + ldl<f32x4>(tp + 32 * 656), b = ldl<f32x4>(tp + 16) + ldl<f32x4>(tp + 32 * 656 + 16);
  *(u32x4*)(out + (32 * mt + m) * 1280 + (160 * nb + 8 * c) * 2) = u32x4{cvtpk(a[0], a[1]), cvtpk(a[2], a[3]), cvtpk(b[0], b[1]), cvtpk(b[2], b[3])};
  STAMP(4);
  PROF_END(2);
}

extern "C" void kernel_launch(void* const* d_in, const int* in_sizes, int n_in, void* d_out, int out_size, void* d_ws, size_t ws_size,
                              hipStream_t stream) {
  const char* x = (const char*)d_in[0]; const char* wqkv = (const char*)d_in[1]; const char* wo = (const char*)d_in[2];
  const char* w1 = (const char*)d_in[4]; const char* b1 = (const char*)d_in[5]; const char* w2 = (const char*)d_in[6]; const char* b2 = (const char*)d_in[7];
  char* ws = (char*)d_ws;
  (void)hipFuncSetAttribute((const void*)k1, hipFuncAttributeMaxDynamicSharedMemorySize, K1_LDS);
  (void)hipFuncSetAttribute((const void*)k2, hipFuncAttributeMaxDynamicSharedMemorySize, K2_LDS);
  (void)hipFuncSetAttribute((const void*)k3, hipFuncAttributeMaxDynamicSharedMemorySize, K3_LDS);
  k1<<<256, 768, K1_LDS, stream>>>(x, wqkv, w1, b1, w2, wo, ws);
  k2<<<256, 512, K2_LDS, stream>>>(x, b2, ws);
  k3<<<256, 640, K3_LDS, stream>>>(ws, (char*)d_out);
}
```

```cpp
#include <hip/hip_runtime.h>
#include <hip/hip_bf16.h>
#include <stdint.h>
#pragma clang diagnostic ignored "-Winline-asm"

using bf16x8 = __attribute__((ext_vector_type(8))) short;
using s16x4 = __attribute__((ext_vector_type(4))) short;
using f32x16 = __attribute__((ext_vector_type(16))) float;
using f32x4 = __attribute__((ext_vector_type(4))) float;
using f32x2 = __attribute__((ext_vector_type(2))) float;
using u32x4 = __attribute__((ext_vector_type(4))) unsigned;
using u32x2 = __attribute__((ext_vector_type(2))) unsigned;
typedef __attribute__((address_space(3))) char* lds_ptr;

#define DEVI __device__ __forceinline__
#define SBAR() __builtin_amdgcn_sched_barrier(0)
#define PIN(x) asm volatile("" : "+v"(x))
#define MFMA16(a, b, c) __builtin_amdgcn_mfma_f32_32x32x16_bf16(a, b, c, 0, 0, 0)
#define MFMA8(a, b, c) __builtin_amdgcn_mfma_f32_32x32x8bf16_1k(a, b, c, 0, 0, 0)
#define MFMAS(a, b, c) __builtin_amdgcn_mfma_f32_16x16x32_bf16(a, b, c, 0, 0, 0)
#define WAIT_BAR(N) asm volatile("s_waitcnt vmcnt(" #N ") lgkmcnt(0)\n\ts_barrier" ::: "memory")
#define LGKM_BAR() asm volatile("s_waitcnt lgkmcnt(0)\n\ts_barrier" ::: "memory")
#define RAW_BAR() asm volatile("s_barrier" ::: "memory")

#define PROF_DECL
#define STAMP(i)
#define PROF_END(kid)

constexpr int SEQ = 2048, DM = 640, FF = 1728;
constexpr int QT = 1280, KVT = 2624;
constexpr size_t WS_QI = 0;
constexpr int KVH = 66 * KVT;
constexpr size_t WS_KVI = WS_QI + 32 * 64 * QT;
constexpr size_t WS_HI = WS_KVI + 32 * KVH;
constexpr size_t WS_W2I = WS_HI + 64 * 108 * 1024;
constexpr size_t WS_OI = WS_W2I + 20 * 148 * 1024;
constexpr float LOG2E = 1.4426950408889634f;
constexpr float QSCALE = 0.22360679774997896f * LOG2E;
constexpr float GC1 = -2.f * 0.7978845608028654f * LOG2E, GC2 = GC1 * 0.044715f;

__host__ __device__ constexpr int recpos(int t) { return t < 2 ? t : 2 + ((t - 2) & 7) + (((t - 2) & 24) << 1) + (((t - 2) & 32) >> 2); }
typedef __bf16 bf16x2_t __attribute__((ext_vector_type(2)));
DEVI unsigned cvtpk(float lo, float hi) { return __builtin_bit_cast(unsigned, __builtin_convertvector(f32x2{lo, hi}, bf16x2_t)); }
DEVI void glds16(const void* g, unsigned lds_base) {
  unsigned sv;
  asm volatile("s_mov_b32 %0, m0\n\ts_mov_b32 m0, %2\n\ts_nop 0\n\tglobal_load_lds_dwordx4 %1, off\n\ts_mov_b32 m0, %0" : "=&s"(sv) : "v"(g), "s"(lds_base) : "memory");
}
DEVI void glds16s(const void* sbase, unsigned voff, unsigned lds_base) {
  asm volatile("s_mov_b32 m0, %2\n\ts_nop 0\n\tglobal_load_lds_dwordx4 %0, %1" :: "v"(voff), "s"(sbase), "s"(lds_base) : "m0", "memory");
}
DEVI s16x4 ldtr(lds_ptr p) { return __builtin_amdgcn_ds_read_tr16_b64_v4i16((__attribute__((address_space(3))) s16x4*)p); }
DEVI bf16x8 ldtr2(lds_ptr p, int off2) { s16x4 a = ldtr(p), b = ldtr(p + off2); return __builtin_shufflevector(a, b, 0, 1, 2, 3, 4, 5, 6, 7); }
template <typename T> DEVI T ldl(lds_ptr p) { return *(__attribute__((address_space(3))) T*)p; }
template <typename T> DEVI void stl(lds_ptr p, T v) { *(__attribute__((address_space(3))) T*)p = v; }
#define AUX16 0
#define AUXF 0
#define AUXH 0
#define AUX8 0
typedef __amdgpu_buffer_rsrc_t rsrc_t;
DEVI rsrc_t mkrs(void* p) { return __builtin_amdgcn_make_buffer_rsrc(p, 0, 0x7fffffff, 0x00020000); }
DEVI void st16(rsrc_t rs, unsigned off, u32x4 v) { __builtin_amdgcn_raw_buffer_store_b128(v, rs, off, 0, AUX16); }
DEVI void st8(rsrc_t rs, unsigned off, u32x2 v) { __builtin_amdgcn_raw_buffer_store_b64(v, rs, off, 0, AUX8); }
DEVI float blo(unsigned d) { return __uint_as_float(d << 16); }
DEVI float bhi(unsigned d) { return __uint_as_float(d & 0xffff0000u); }
DEVI u32x4 pack8(const float* v) { return u32x4{cvtpk(v[0], v[1]), cvtpk(v[2], v[3]), cvtpk(v[4], v[5]), cvtpk(v[6], v[7])}; }
DEVI float gelu(float u) {
  float z = u * __builtin_fmaf(GC2, u * u, GC1);
  return u * __builtin_amdgcn_rcpf(1.f + __builtin_amdgcn_exp2f(z));
}

constexpr int K1_STG = 49152, K1_SP = 3 * K1_STG, K1_LDS = K1_SP + 16384;
constexpr int K1_RS = K1_SP, K1_CS = K1_SP + 1024;

struct K1Frag { bf16x8 x[2], w[2]; };

template <bool VF>
DEVI void k1_main(f32x16 (&acc)[2][2], f32x4 (&cs)[2], lds_ptr lds, int xa0, int wa0, bf16x8 sel) {
  lds_ptr xp[4], wp[2];
#pragma unroll
  for (int p = 0; p < 4; ++p) xp[p] = lds + (xa0 ^ (p * 32));
  wp[0] = lds + 16384 + wa0; wp[1] = lds + 16384 + (wa0 ^ 4096);
  K1Frag A, B;
  B.x[0] = B.x[1] = B.w[0] = B.w[1] = bf16x8{};
#define K1_LD(F, p) do { F.x[0] = ldl<bf16x8>(xp[p]); F.w[0] = ldtr2(wp[(p) & 1] + ((p) >> 1) * 8192, 1024); F.x[1] = ldl<bf16x8>(xp[p] + 4096); \
    F.w[1] = ldtr2(wp[(p) & 1] + ((p) >> 1) * 8192 + 16384, 1024); } while (0)
#define K1_MM(F) do { \
    if (VF) { acc[0][0] = MFMA16(F.x[0], F.w[0], acc[0][0]); acc[0][1] = MFMA16(F.x[1], F.w[0], acc[0][1]); } \
    else { acc[0][0] = MFMA16(F.w[0], F.x[0], acc[0][0]); acc[0][1] = MFMA16(F.w[0], F.x[1], acc[0][1]); } \
    acc[1][0] = MFMA16(F.w[1], F.x[0], acc[1][0]); acc[1][1] = MFMA16(F.w[1], F.x[1], acc[1][1]); } while (0)
#define K1_CSUM(F) do { cs[0] = MFMAS(F.w[0], sel, cs[0]); cs[1] = MFMAS(F.w[1], sel, cs[1]); } while (0)
  for (int s = 0; s < 10; ++s) {
    LGKM_BAR();
    K1_LD(A, 0); SBAR();
    K1_MM(B); SBAR();
    K1_LD(B, 1); SBAR();
    K1_MM(A); K1_CSUM(A); SBAR();
    K1_LD(A, 2); SBAR();
    K1_MM(B); SBAR();
    K1_LD(B, 3); SBAR();
    K1_MM(A); K1_CSUM(A); SBAR();
    const int dl = (s % 3 == 2) ? -2 * K1_STG : K1_STG;
#pragma unroll
    for (int p = 0; p < 4; ++p) xp[p] += dl;
    wp[0] += dl; wp[1] += dl;
  }
  K1_MM(B);
}

__global__ __launch_bounds__(768, 3) void k1(const char* __restrict__ x, const char* __restrict__ wqkv, const char* __restrict__ w1,
                                             const char* __restrict__ b1, const char* __restrict__ w2, const char* __restrict__ wo, char* __restrict__ ws) {
  extern __shared__ __attribute__((aligned(16))) char smem[];
  const lds_ptr lds = (lds_ptr)smem;
  const unsigned lds0 = (unsigned)(uintptr_t)smem;
  const int lane = threadIdx.x & 63, w = __builtin_amdgcn_readfirstlane(threadIdx.x >> 6), r = lane & 31, hh = lane >> 5;
  const int bid = blockIdx.x, x8 = bid & 7, j = bid >> 3;
  const int q4 = (lane & 15) >> 2, cb = 32 * ((lane >> 4) & 1) + 8 * (lane & 3);
  const rsrc_t rs_ = mkrs(ws);
#define WOFF(p) (unsigned)((p) - ws)
  PROF_DECL STAMP(0);
  if (j >= 30) {
    const int hid = 2 * x8 + (j - 30), gw = 12 * hid + w;
#pragma unroll
    for (int rd = 0; rd < 2; ++rd) {
      char* dst[4]; int ust[4];
#pragma unroll
      for (int i = 0; i < 4; ++i) {
        int b = gw + 192 * (4 * rd + i); b = b < 1480 ? b : gw;
        const bool isw2 = b < 1080;
        const int bb = isw2 ? b : b - 1080, v = bb % 10, ks = bb / 10, slot = lane >> 3;
        const int row0 = isw2 ? 16 * ks + slot : (ks < 32 ? 20 * ks + slot : 20 * (4 * (ks - 32) + 2 * (slot >> 2)) + 16 + (slot & 3));
        const int rstep = (isw2 || ks < 32) ? 8 : 20;
        const char* src = (isw2 ? w2 : wo) + row0 * 1280 + v * 128 + (lane & 7) * 16;
        glds16(src, lds0 + w * 8192 + i * 2048); glds16(src + rstep * 1280, lds0 + w * 8192 + i * 2048 + 1024);
        const int u0 = 2 * v, u1 = 2 * v + 1;
        const int o0 = (u0 / 5) * 148 * 5120 + (u0 % 5) * 1024, o1 = (u1 / 5) * 148 * 5120 + (u1 % 5) * 1024;
        ust[i] = o1 - o0;
        dst[i] = ws + WS_W2I + o0 + (b / 10) * 5120 + lane * 16;
      }
      if (rd == 0) {
        const int t = hid * 768 + threadIdx.x;
        const unsigned cv = (t & 1) ? 0u : 0x3f803f80u;
        if (t < 8448) *(u32x4*)(ws + WS_KVI + (size_t)(t >> 2) * KVT + 1280 + 640 + ((t >> 1) & 1) * 672 + (t & 1) * 16) = u32x4{cv, cv, cv, cv};
      }
      for (int i = hid * 768 + threadIdx.x + rd * 81920; i < 81920 + rd * 81920; i += 12288)
        __builtin_amdgcn_raw_buffer_store_b128(u32x4{~0u, ~0u, ~0u, ~0u}, rs_, WS_OI + i * 16, 0, AUXF);
      asm volatile("s_waitcnt vmcnt(0)" ::: "memory");
      const lds_ptr rp = lds + w * 8192 + (4 * hh + q4) * 128 + cb;
#pragma unroll
      for (int i = 0; i < 4; ++i) {
        const bf16x8 a = ldtr2(rp + i * 2048, 1024), b = ldtr2(rp + i * 2048 + 64, 1024);
        __builtin_amdgcn_raw_buffer_store_b128(__builtin_bit_cast(u32x4, a), rs_, WOFF(dst[i]), 0, AUXH);
        __builtin_amdgcn_raw_buffer_store_b128(__builtin_bit_cast(u32x4, b), rs_, WOFF(dst[i] + ust[i]), 0, AUXH);
      }
      asm volatile("s_waitcnt lgkmcnt(0)" ::: "memory");
    }
    PROF_END(0);
    return;
  }
  const int L = 30 * x8 + j, mhalf = L >= 120, Lr = L - 120 * mhalf, n = Lr >> 3, mg = 8 * mhalf + (Lr & 7);
  if (w >= 8) {
    const int lw = w - 8;
    unsigned xo[4], qo[4], mo[4];
    const int ch = (lane & 15) ^ ((lane >> 4) << 2), chm = n >= 13 ? (ch & 7) : ch;
#pragma unroll
    for (int i = 0; i < 4; ++i) {
      xo[i] = (32 * lw + 8 * i + (lane >> 3)) * 1280 + (((lane & 7) ^ ((4 * i + (lane >> 4)) & 7)) * 16);
      qo[i] = (16 * lw + 4 * i + (lane >> 4)) * 3840 + ch * 16;
      mo[i] = (16 * lw + 4 * i + (lane >> 4)) * 3456 + chm * 16;
    }
    const char* xb = x + mg * (128 * 1280);
    const char* qb = wqkv + n * 256;
    const char* mb = w1 + (n < 13 ? n : 13) * 256;
    unsigned nb = lds0 + lw * 4096;
#pragma unroll
    for (int s = 0; s < 2; ++s) {
#pragma unroll
      for (int i = 0; i < 4; ++i) { glds16s(xb, xo[i], nb + i * 1024); glds16s(qb, qo[i], nb + 16384 + i * 1024); glds16s(mb, mo[i], nb + 32768 + i * 1024); }
      xb += 128; qb += 64 * 3840; mb += 64 * 3456; nb += K1_STG;
    }
    lds_ptr sp[4];
#pragma unroll
    for (int i = 0; i < 4; ++i) sp[i] = lds + (32 * lw + r) * 128 + (((2 * i + hh) ^ ((r >> 1) & 7)) * 16);
    float s0 = 0.f, s1 = 0.f, q0 = 0.f, q1 = 0.f;
    STAMP(1);
    for (int s = 0; s < 10; ++s) {
      if (s < 9) WAIT_BAR(12); else WAIT_BAR(0);
      if (s == 0) STAMP(2);
      u32x4 d[4];
#pragma unroll
      for (int i = 0; i < 4; ++i) d[i] = ldl<u32x4>(sp[i]);
      SBAR();
#define K1_ST(v) do { const float lo = blo(v), hi = bhi(v); \
        asm("v_add_f32 %0, %0, %2\n\tv_fmac_f32 %1, %2, %2" : "+v"(s0), "+v"(q0) : "v"(lo)); asm("v_add_f32 %0, %0, %2\n\tv_fmac_f32 %1, %2, %2" : "+v"(s1), "+v"(q1) : "v"(hi)); } while (0)
      if (s < 8) {
#pragma unroll
        for (int i = 0; i < 4; ++i) {
          glds16s(xb, xo[i], nb + i * 1024); K1_ST(d[i][0]); SBAR();
          glds16s(qb, qo[i], nb + 16384 + i * 1024); K1_ST(d[i][1]); SBAR();
          glds16s(mb, mo[i], nb + 32768 + i * 1024); K1_ST(d[i][2]); K1_ST(d[i][3]); SBAR();
        }
        xb += 128; qb += 64 * 3840; mb += 64 * 3456;
        nb = (s % 3 == 0) ? nb - 2 * K1_STG : nb + K1_STG;
      } else {
#pragma unroll
        for (int i = 0; i < 4; ++i) { K1_ST(d[i][0]); K1_ST(d[i][1]); K1_ST(d[i][2]); K1_ST(d[i][3]); }
      }
      const int dl = (s % 3 == 2) ? -2 * K1_STG : K1_STG;
#pragma unroll
      for (int i = 0; i < 4; ++i) sp[i] += dl;
    }
    STAMP(3);
    s0 += s1; q0 += q1;
    auto a = __builtin_amdgcn_permlane32_swap(__float_as_uint(s0), __float_as_uint(q0), false, false);
    const float t = __uint_as_float(a[0]) + __uint_as_float(a[1]);
    auto b = __builtin_amdgcn_permlane32_swap(__float_as_uint(t), __float_as_uint(t), false, false);
    const float mean = __uint_as_float(b[0]) * (1.f / 640.f);
    const float var = __builtin_fmaxf(__builtin_fmaf(-mean, mean, __uint_as_float(b[1]) * (1.f / 640.f)), 0.f);
    const float rstd = __builtin_amdgcn_rsqf(var + 1e-5f);
    stl<float>(lds + K1_RS + hh * 512 + (32 * lw + r) * 4, hh ? -mean * rstd : rstd);
    LGKM_BAR();
    STAMP(4);
    PROF_END(0);
    return;
  }
  const int mh = w & 1, np = w >> 1;
  const int fb = 128 * n + 32 * np;
  const bool mlp_ok = fb < FF;
  u32x2 bias[4];
#pragma unroll
  for (int i = 0; i < 4; ++i) bias[i] = *(const u32x2*)(b1 + (mlp_ok ? fb + 8 * i + 4 * hh : 0) * 2);
  const int xa0 = (64 * mh + r) * 128 + (((2 * mh + hh) ^ ((r >> 1) & 7)) * 16);
  const int wa0 = (16 * mh + 8 * hh + q4) * 256 + ((64 * np + cb) ^ (q4 << 6));
  const short one = ((lane & 15) == ((lane >> 4) & 1)) ? (short)0x3f80 : (short)0;
  const bf16x8 sel = {one, one, one, one, one, one, one, one};
  f32x16 acc[2][2] = {};
  f32x4 cs[2] = {};
  STAMP(1);
  if (n < 10) k1_main<false>(acc, cs, lds, xa0, wa0, sel);
  else k1_main<true>(acc, cs, lds, xa0, wa0, sel);
  STAMP(2);
  PIN(bias[0]); PIN(bias[1]); PIN(bias[2]); PIN(bias[3]);
  const lds_ptr CS = lds + K1_CS + np * 256;
  if ((lane & 15) < 2) {
    stl<f32x4>(CS + mh * 1024 + (16 * (lane & 1) + 4 * (lane >> 4)) * 4, cs[0]);
    stl<f32x4>(CS + mh * 1024 + 128 + (16 * (lane & 1) + 4 * (lane >> 4)) * 4, cs[1]);
  }
  LGKM_BAR();
  const int mt0 = 4 * mg + 2 * mh;
  const lds_ptr RS = lds + K1_RS + 256 * mh;
  float rs[2], nm[2];
#pragma unroll
  for (int t = 0; t < 2; ++t) { rs[t] = ldl<float>(RS + (32 * t + r) * 4); nm[t] = ldl<float>(RS + 512 + (32 * t + r) * 4); }
  if (n < 10) {
    const bool isq = n < 5;
    const float sc = isq ? QSCALE : 1.f;
    const int tb = isq ? QT : KVT, hb = isq ? 64 * QT : KVH;
    char* base = ws + (isq ? WS_QI + mt0 * QT : WS_KVI + recpos(mt0) * KVT);
    const int q0 = 32 * (isq ? n : n - 5) + 8 * np + hh;
#pragma unroll
    for (int g = 0; g < 4; ++g) {
      const f32x4 c = ldl<f32x4>(CS + (8 * g + 4 * hh) * 4) + ldl<f32x4>(CS + 1024 + (8 * g + 4 * hh) * 4);
      const int qd = q0 + 2 * g, hd = (qd * 205) >> 10, dq = qd - 5 * hd;
      char* dst = base + hd * hb + (dq < 4 ? (dq & 1) * 512 + r * 16 + (dq >> 1) * 8 : 1024 + r * 8);
#pragma unroll
      for (int t = 0; t < 2; ++t) {
        const float a = rs[t] * sc, b = nm[t] * sc;
        float v[4];
#pragma unroll
        for (int e = 0; e < 4; ++e) v[e] = __builtin_fmaf(a, acc[0][t][4 * g + e], b * c[e]);
        st8(rs_, WOFF(dst + t * tb), u32x2{cvtpk(v[0], v[1]), cvtpk(v[2], v[3])});
      }
    }
  } else {
    const float c = ldl<float>(CS + r * 4) + ldl<float>(CS + 1024 + r * 4);
    const int col = 128 * (n - 10) + 32 * np + r, hd = (col * 3277) >> 16, d = col - 20 * hd;
    char* dst = ws + WS_KVI + hd * KVH + 1280 + hh * 320 + d * 16;
    const int ro[3] = {recpos(mt0 + 1) * KVT, recpos(mt0 + 2) * KVT, recpos(mt0 + 2) * KVT + KVT};
#pragma unroll
    for (int t = 0; t < 2; ++t) {
      float v[16];
#pragma unroll
      for (int i = 0; i < 4; ++i) {
        const f32x4 a = ldl<f32x4>(RS + (32 * t + 8 * i + 4 * hh) * 4), b = ldl<f32x4>(RS + 512 + (32 * t + 8 * i + 4 * hh) * 4);
#pragma unroll
        for (int e = 0; e < 4; ++e) v[4 * i + e] = __builtin_fmaf(a[e], acc[0][t][4 * i + e], b[e] * c);
      }
      st16(rs_, WOFF(dst + ro[t]), pack8(v)); st16(rs_, WOFF(dst + ro[t + 1] + 672), pack8(v + 8));
    }
  }
  STAMP(3);
  if (mlp_ok) {
    f32x4 c4[4];
#pragma unroll
    for (int i = 0; i < 4; ++i) c4[i] = ldl<f32x4>(CS + 128 + (8 * i + 4 * hh) * 4) + ldl<f32x4>(CS + 1024 + 128 + (8 * i + 4 * hh) * 4);
    char* hdst = ws + WS_HI + (size_t)(mt0 * 108 + 8 * n + 2 * np) * 1024 + lane * 16;
#pragma unroll
    for (int t = 0; t < 2; ++t) {
      float v[16];
#pragma unroll
      for (int i = 0; i < 16; ++i) {
        const unsigned bd = bias[i >> 2][(i >> 1) & 1];
        v[i] = gelu(__builtin_fmaf(rs[t], acc[1][t][i], __builtin_fmaf(nm[t], c4[i >> 2][i & 3], (i & 1) ? bhi(bd) : blo(bd))));
      }
      st16(rs_, WOFF(hdst + t * 108 * 1024), pack8(v));
      st16(rs_, WOFF(hdst + t * 108 * 1024 + 1024), pack8(v + 8));
    }
  }
  STAMP(4);
  PROF_END(0);
}

constexpr int K2_CH = 16 * KVT, K2_W0 = K2_CH, K2_W1 = K2_W0 + 4 * 21504, K2_LDS = K2_W1 + 4 * 8704;

DEVI float max16(const f32x16& s) {
  float a = __builtin_fmaxf(__builtin_fmaxf(s[0], s[1]), s[2]), b = __builtin_fmaxf(__builtin_fmaxf(s[3], s[4]), s[5]);
  a = __builtin_fmaxf(__builtin_fmaxf(a, s[6]), s[7]); b = __builtin_fmaxf(__builtin_fmaxf(b, s[8]), s[9]);
  a = __builtin_fmaxf(__builtin_fmaxf(a, s[10]), s[11]); b = __builtin_fmaxf(__builtin_fmaxf(b, s[12]), s[13]);
  a = __builtin_fmaxf(__builtin_fmaxf(a, s[14]), s[15]); a = __builtin_fmaxf(a, b);
  auto t = __builtin_amdgcn_permlane32_swap(__float_as_uint(a), __float_as_uint(a), false, false);
  return __builtin_fmaxf(__uint_as_float(t[0]), __uint_as_float(t[1]));
}

#define VZ 656
struct K2S {
  f32x16 s[2][2], o[2], acc[3];
  u32x4 p[2][2];
  bf16x8 qm[2], km, v0, v1, f[2][4], wf[30];
  s16x4 qr[2], kr;
  lds_ptr kmp, krp, vp;
  const char *hp, *wp, *src, *wo;
  unsigned dst, l16, woff, wdst;
  unsigned long long msk;
};
#define BC(p) __builtin_bit_cast(bf16x8, p)
enum { Q8A, Q8B, P1A, P1B, Q16A, Q16B, P0A, P0B, G0, G1, G2, FL };
__host__ __device__ constexpr int k2_slot(int ng, int op) {
  constexpr int pos0[8] = {0, 6, 12, 18, 24, 30, 36, 42}, id0[8] = {Q8A, Q8B, P1A, P1B, Q16A, Q16B, P0A, P0B};
  constexpr int pos2[10] = {0, 4, 9, 14, 19, 24, 28, 33, 38, 43}, id2[10] = {Q8A, Q8B, G0, P1A, P1B, Q16A, Q16B, G1, P0A, P0B};
  constexpr int pos3[11] = {0, 4, 9, 13, 17, 22, 26, 30, 35, 39, 43}, id3[11] = {Q8A, Q8B, G0, P1A, P1B, G1, Q16A, Q16B, G2, P0A, P0B};
  if (op == 41) return FL;
  for (int i = 0; i < 8 + ng; ++i) if ((ng == 0 ? pos0[i] : ng == 2 ? pos2[i] : pos3[i]) == op) return ng == 0 ? id0[i] : ng == 2 ? id2[i] : id3[i];
  return -1;
}
template <int J> DEVI void k2_fill(K2S& z) {
  if (J < 5) glds16s(z.src + J * 8192, z.l16, z.dst + J * 8192);
  else asm volatile("s_mov_b32 m0, %2\n\ts_mov_b64 exec, %3\n\tglobal_load_lds_dwordx4 %0, %1\n\ts_mov_b64 exec, -1"
                    :: "v"(z.l16), "s"(z.src - z.woff + 40960), "s"(z.dst - z.woff + 40960), "s"(z.msk) : "m0", "memory");
}
template <int NU, int S> DEVI void k2_ldw(K2S& z, int u) {
  if (u == 0) asm volatile("global_load_dwordx4 %0, %1, %2" : "=v"(z.f[S][0]) : "v"(z.l16), "s"(z.wp) : "memory");
  if (u == 1) asm volatile("global_load_dwordx4 %0, %1, %2 offset:1024" : "=v"(z.f[S][1]) : "v"(z.l16), "s"(z.wp) : "memory");
  if (u == 2) asm volatile("global_load_dwordx4 %0, %1, %2 offset:2048" : "=v"(z.f[S][2]) : "v"(z.l16), "s"(z.wp) : "memory");
  if (u == NU - 1) { asm volatile("global_load_dwordx4 %0, %1, %2" : "=v"(z.f[S][3]) : "v"(z.l16), "s"(z.hp) : "memory"); z.hp += 1024; z.wp += 5120; }
}
template <int NU, int F> DEVI void k2_ldo(K2S& z) {
  constexpr int off = (F / NU) * 5120 + (F % NU) * 1024;
  if constexpr (F < (NU == 3 ? 21 : 8)) glds16s(z.wo + off, z.l16, z.wdst + F * 1024);
  else if constexpr (F < 10 * NU) asm volatile("global_load_dwordx4 %0, %1, %2" : "=v"(z.wf[F]) : "v"(z.l16), "s"(z.wo + off) : "memory");
}
template <int NU, int I, int GM, bool FILL, int VM, int OP = 0> DEVI void k2_tile(K2S& z) {
  if constexpr (!FILL && I < 3 && (OP == 37 || OP == 41 || OP == 45)) k2_ldo<NU, 3 * I + (OP - 37) / 4>(z);
  if constexpr (!FILL && I >= 3 && OP % 8 == 3 && (NU == 3 || OP < 24)) k2_ldo<NU, 9 + (NU == 3 ? 6 : 3) * (I - 3) + OP / 8>(z);
  constexpr int P = I & 1, m = k2_slot(GM ? NU : 0, OP), off = I * KVT;
  f32x16 (&S)[2] = z.s[P], (&N)[2] = z.s[P ^ 1];
  if constexpr (m == Q8A) N[0] = MFMA8(z.kr, z.qr[0], f32x16{});
  if constexpr (m == Q8B) { N[1] = MFMA8(z.kr, z.qr[1], f32x16{}); SBAR(); z.kr = ldl<s16x4>(z.krp + off); }
  if constexpr (m == P1A) z.o[0] = MFMA16(z.v1, BC(z.p[1][0]), z.o[0]);
  if constexpr (m == P1B) { z.o[1] = MFMA16(z.v1, BC(z.p[1][1]), z.o[1]); SBAR(); z.v1 = ldl<bf16x8>(z.vp + off + 672); }
  if constexpr (m == Q16A) N[0] = MFMA16(z.km, z.qm[0], N[0]);
  if constexpr (m == Q16B) { N[1] = MFMA16(z.km, z.qm[1], N[1]); SBAR(); z.km = ldl<bf16x8>(z.kmp + off); }
  if constexpr (m == P0A) z.o[0] = MFMA16(z.v0, BC(z.p[0][0]), z.o[0]);
  if constexpr (m == P0B) { z.o[1] = MFMA16(z.v0, BC(z.p[0][1]), z.o[1]); SBAR(); z.v0 = ldl<bf16x8>(z.vp + off); }
  if constexpr (m == G0) {
    if constexpr (NU == 3) asm volatile("s_waitcnt vmcnt(%4)" : "+v"(z.f[P][0]), "+v"(z.f[P][1]), "+v"(z.f[P][2]), "+v"(z.f[P][3]) : "n"(VM) : "memory");
    else asm volatile("s_waitcnt vmcnt(%3)" : "+v"(z.f[P][0]), "+v"(z.f[P][1]), "+v"(z.f[P][3]) : "n"(VM) : "memory");
  }
  if constexpr (m >= G0 && m <= G2) {
    z.acc[m - G0] = MFMA16(z.f[P][m - G0], z.f[P][3], z.acc[m - G0]); SBAR();
    if (GM == 2) k2_ldw<NU, P>(z, m - G0);
  }
  if constexpr (m == FL && FILL && I < 6) k2_fill<I>(z);
  SBAR();
  constexpr int pr = OP / 3, k = OP % 3;
  if constexpr (k < 2) { constexpr int e = (pr >> 3) * 8 + (pr & 3) * 2 + k; S[(pr >> 2) & 1][e] = __builtin_amdgcn_exp2f(S[(pr >> 2) & 1][e]); }
  else if constexpr (pr == 0) z.p[1][1][3] = cvtpk(N[1][14], N[1][15]);
  else { constexpr int q = pr - 1, e = (q >> 3) * 8 + (q & 3) * 2; z.p[q >> 3][(q >> 2) & 1][q & 3] = cvtpk(S[(q >> 2) & 1][e], S[(q >> 2) & 1][e + 1]); }
  SBAR();
  if constexpr (OP < 47) k2_tile<NU, I, GM, FILL, VM, OP + 1>(z);
}
#define APRIO(x) __builtin_amdgcn_s_setprio(x)
template <int NU> DEVI void k2_loop(K2S& z) {
  constexpr int L = NU + 1;
  k2_ldw<NU, 0>(z, 0); k2_ldw<NU, 0>(z, 1); if (NU == 3) k2_ldw<NU, 0>(z, 2);
  k2_ldw<NU, 1>(z, 0); k2_ldw<NU, 1>(z, 1); if (NU == 3) k2_ldw<NU, 1>(z, 2);
  for (int c = 0; c < 3; ++c) {
    asm volatile("s_waitcnt vmcnt(%0) lgkmcnt(0)\n\ts_barrier" :: "n"(2 * L) : "memory");
    z.src += K2_CH;
    z.dst = (c & 1) ? z.dst + K2_CH : z.dst - K2_CH;
    APRIO(3);
    k2_tile<NU, 0, 2, true, L>(z); k2_tile<NU, 1, 2, true, L + 1>(z); k2_tile<NU, 2, 2, true, L + 2>(z);
    APRIO(2);
    k2_tile<NU, 3, 2, true, L + 2>(z); k2_tile<NU, 4, 2, true, L + 2>(z); k2_tile<NU, 5, 2, true, L + 2>(z);
    APRIO(1);
    k2_tile<NU, 6, 2, true, L + 2>(z);
    APRIO(0);
    k2_tile<NU, 7, 2, true, L + 1>(z);
    const int dl = (c & 1) ? K2_CH : -K2_CH;
    z.kmp += dl; z.krp += dl; z.vp += dl;
  }
  asm volatile("s_waitcnt vmcnt(%0) lgkmcnt(0)\n\ts_barrier" :: "n"(2 * L) : "memory");
  APRIO(3);
  k2_tile<NU, 0, 2, false, L>(z); k2_tile<NU, 1, 1, false, L + 3>(z); k2_tile<NU, 2, 1, false, 6>(z);
  APRIO(2);
  k2_tile<NU, 3, 0, false, 0>(z); k2_tile<NU, 4, 0, false, 0>(z); k2_tile<NU, 5, 0, false, 0>(z);
  APRIO(1);
  k2_tile<NU, 6, 0, false, 0>(z);
  APRIO(0);
  k2_tile<NU, 7, 0, false, 0>(z);
  z.p[1][1][3] = cvtpk(z.s[1][1][14], z.s[1][1][15]);
  z.o[0] = MFMA16(z.v1, BC(z.p[1][0]), z.o[0]); z.o[1] = MFMA16(z.v1, BC(z.p[1][1]), z.o[1]);
}

template <int kh> DEVI void k2_body(const char* __restrict__ x, const char* __restrict__ b2, char* __restrict__ ws, char* __restrict__ out, const int w) {
  extern __shared__ __attribute__((aligned(16))) char smem[];
  const lds_ptr lds = (lds_ptr)smem;
  const unsigned lds0 = (unsigned)(uintptr_t)smem;
  const int lane = threadIdx.x & 63, r = lane & 31, hh = lane >> 5;
  const int bid = blockIdx.x, xcd = bid & 7;
  const unsigned l16 = lane * 16;
  PROF_DECL STAMP(0);
  const int h = 4 * xcd + ((bid >> 3) & 3), kq = w & 3, mt = 8 * (bid >> 5) + 2 * kq;
  const int gmt = 8 * (bid >> 5) + 2 * (xcd >> 1) + ((bid >> 4) & 1), nb = 2 * (xcd & 1) + ((bid >> 3) & 1);
  const char* kv = ws + WS_KVI + (size_t)h * KVH;
  const char* qp = ws + WS_QI + (size_t)(h * 64 + mt) * QT;
  const char* k0p = kv + (kh ? recpos(32) * KVT : 0);
  const unsigned r8 = 1024 + r * 8, vo = 1280 + (r < 20 ? hh * 320 + r * 16 : r == 20 ? 640 : VZ);
  K2S z;
  bf16x8 kmf, km0; s16x4 krf, kr0;
  z.l16 = l16; z.woff = w * 1024; z.src = kv + 2 * KVT + z.woff; z.dst = lds0 + K2_CH + z.woff; z.msk = 0xffull << (8 * w);
  z.hp = ws + WS_HI + (size_t)(gmt * 108 + 27 * kq) * 1024;
  z.wp = ws + WS_W2I + (size_t)(nb * 148 + 27 * kq) * 5120 + kh * 3072;
  z.wo = ws + WS_W2I + (size_t)(nb * 148 + 108 + 10 * kq) * 5120 + kh * 3072;
  const int wreg = kh ? K2_W1 + kq * 8704 : K2_W0 + kq * 21504;
  z.wdst = lds0 + wreg;
  asm volatile("global_load_dwordx4 %0, %11, %13\n\tglobal_load_dwordx2 %1, %12, %13\n\tglobal_load_dwordx4 %2, %11, %14\n\tglobal_load_dwordx2 %3, %12, %14\n\t"
               "global_load_dwordx4 %4, %11, %13 offset:1280\n\tglobal_load_dwordx2 %5, %12, %13 offset:1280\n\t"
               "global_load_dwordx4 %6, %11, %15\n\tglobal_load_dwordx2 %7, %12, %15\n\t"
               "global_load_dwordx2 %8, %12, %15 offset:2624\n\tglobal_load_dwordx4 %9, %11, %15 offset:2624\n\tglobal_load_dwordx4 %10, %16, %15 offset:2624"
               : "=&v"(z.qm[0]), "=&v"(z.qr[0]), "=&v"(kmf), "=&v"(krf), "=&v"(z.qm[1]), "=&v"(z.qr[1]), "=&v"(km0), "=&v"(kr0), "=&v"(z.kr), "=&v"(z.km), "=&v"(z.v0)
               : "v"(l16), "v"(r8), "s"(qp), "s"(kv), "s"(k0p), "v"(vo) : "memory");
  k2_fill<0>(z); k2_fill<1>(z); k2_fill<2>(z); k2_fill<3>(z); k2_fill<4>(z); k2_fill<5>(z);
  asm volatile("s_waitcnt vmcnt(11)" : "+v"(z.qm[0]), "+v"(z.qr[0]), "+v"(kmf), "+v"(krf), "+v"(z.qm[1]), "+v"(z.qr[1]) :: "memory");
  if (hh) { z.qr[0] = s16x4{}; z.qr[1] = s16x4{}; }
  {
    f32x16 sA = MFMA8(krf, z.qr[0], f32x16{}), sB = MFMA8(krf, z.qr[1], f32x16{});
    sA = MFMA16(kmf, z.qm[0], sA); sB = MFMA16(kmf, z.qm[1], sB);
    const unsigned mA = cvtpk(-max16(sA), 0.f), mB = cvtpk(-max16(sB), 0.f);
    if (hh) { z.qr[0][0] = (short)mA; z.qr[1][0] = (short)mB; }
  }
  SBAR();
  asm volatile("s_waitcnt vmcnt(6)" : "+v"(km0), "+v"(kr0), "+v"(z.kr), "+v"(z.km), "+v"(z.v0) :: "memory");
  if (hh) { kr0 = s16x4{0x3f80, 0x3f80, 0x3f80, 0x3f80}; z.kr = kr0; }
  z.s[0][0] = MFMA8(kr0, z.qr[0], f32x16{}); z.s[0][1] = MFMA8(kr0, z.qr[1], f32x16{});
  z.s[0][0] = MFMA16(km0, z.qm[0], z.s[0][0]); z.s[0][1] = MFMA16(km0, z.qm[1], z.s[0][1]);
  z.s[1][0] = z.s[1][1] = z.o[0] = z.o[1] = z.acc[0] = z.acc[1] = z.acc[2] = f32x16{};
  z.v1 = bf16x8{};
  z.p[1][0] = z.p[1][1] = u32x4{};
  z.kmp = lds + K2_CH + kh * 8 * KVT + l16; z.krp = lds + K2_CH + kh * 8 * KVT + (hh ? 1920 : r8); z.vp = lds + K2_CH + kh * 8 * KVT + vo;
  SBAR();
  STAMP(1);
  k2_loop<3 - kh>(z);
  STAMP(3);
  constexpr int nu = 3 - kh, nl = kh ? 8 : 21;
  if constexpr (kh) asm volatile("s_waitcnt vmcnt(0)" : "+v"(z.wf[8]), "+v"(z.wf[9]), "+v"(z.wf[10]), "+v"(z.wf[11]), "+v"(z.wf[12]), "+v"(z.wf[13]),
                                 "+v"(z.wf[14]), "+v"(z.wf[15]), "+v"(z.wf[16]), "+v"(z.wf[17]), "+v"(z.wf[18]), "+v"(z.wf[19]) :: "memory");
  else asm volatile("s_waitcnt vmcnt(0)" : "+v"(z.wf[21]), "+v"(z.wf[22]), "+v"(z.wf[23]), "+v"(z.wf[24]), "+v"(z.wf[25]), "+v"(z.wf[26]),
                    "+v"(z.wf[27]), "+v"(z.wf[28]), "+v"(z.wf[29]) :: "memory");
  const int t = threadIdx.x;
  const int go[3] = {(32 * gmt + (t >> 4)) * 1280 + (t & 15) * 8, 192, (32 * gmt + ((t & 255) >> 3)) * 1280 + 128 + (t & 7) * 8};
  u32x2 xv[3], bv[3];
  xv[0] = *(const u32x2*)(x + 320 * nb + go[0]); xv[1] = *(const u32x2*)(x + 320 * nb + go[0] + 192);
  bv[0] = *(const u32x2*)(b2 + 320 * nb + (t & 15) * 8); bv[1] = *(const u32x2*)(b2 + 320 * nb + (t & 15) * 8 + 192);
  if (kh) { xv[2] = *(const u32x2*)(x + 320 * nb + go[2]); bv[2] = *(const u32x2*)(b2 + 320 * nb + 128 + (t & 7) * 8); }
  f32x16 o = kh ? z.o[1] : z.o[0];
  {
    const f32x16 g = kh ? z.o[0] : z.o[1];
    const lds_ptr xw = lds + w * 3072 + l16, xr = lds + (w ^ 4) * 3072 + l16;
    LGKM_BAR();
#pragma unroll
    for (int i = 0; i < 3; ++i) stl<f32x4>(xw + i * 1024, f32x4{g[4 * i], g[4 * i + 1], g[4 * i + 2], g[4 * i + 3]});
    LGKM_BAR();
#pragma unroll
    for (int i = 0; i < 3; ++i) { const f32x4 q = ldl<f32x4>(xr + i * 1024); o[4 * i] += q[0]; o[4 * i + 1] += q[1]; o[4 * i + 2] += q[2]; o[4 * i + 3] += q[3]; }
  }
  auto lt = __builtin_amdgcn_permlane32_swap(__float_as_uint(o[8]), __float_as_uint(o[8]), false, false);
  float l = __uint_as_float(lt[1]);
  float om = 0.f;
#pragma unroll
  for (int i = 0; i < 12; ++i) om = __builtin_fmaxf(om, __builtin_fabsf(o[i]));
  const bool bad = !(l >= 0x1p-30f && l <= 0x1p60f && om <= 0x1p126f);
  if (__builtin_amdgcn_ballot_w64(bad) != 0) {
    const bf16x8 qm = kh ? z.qm[1] : z.qm[0];
    s16x4 qr = kh ? z.qr[1] : z.qr[0];
    if (hh) qr = s16x4{};
    float m = -__builtin_inff();
    o = f32x16{};
    for (int t = 0; t < 64; ++t) {
      const char* tp = kv + recpos(t) * KVT;
      const bf16x8 a = *(const bf16x8*)(tp + l16), va = *(const bf16x8*)(kv + recpos(t + 1) * KVT + vo), vb = *(const bf16x8*)(kv + recpos(t + 2) * KVT + vo + 672);
      const s16x4 b = *(const s16x4*)(tp + r8);
      f32x16 s = MFMA8(b, qr, f32x16{}); s = MFMA16(a, qm, s);
      const float mn = __builtin_fmaxf(m, max16(s)), al = __builtin_amdgcn_exp2f(m - mn);
      m = mn;
      float p[16];
#pragma unroll
      for (int i = 0; i < 16; ++i) { p[i] = __builtin_amdgcn_exp2f(s[i] - mn); o[i] *= al; }
      o = MFMA16(va, BC(pack8(p)), o);
      o = MFMA16(vb, BC(pack8(p + 8)), o);
    }
    lt = __builtin_amdgcn_permlane32_swap(__float_as_uint(o[8]), __float_as_uint(o[8]), false, false);
    l = __uint_as_float(lt[1]);
  }
  const float il = __builtin_amdgcn_rcpf(l);
  float v[12];
#pragma unroll
  for (int i = 0; i < 12; ++i) v[i] = o[i] * il;
  {
    const rsrc_t rs = mkrs(ws + WS_OI + (size_t)(mt + kh) * 40 * 1024);
    unsigned d[6];
#pragma unroll
    for (int i = 0; i < 6; ++i) d[i] = __builtin_elementwise_min(cvtpk(v[2 * i], v[2 * i + 1]), 0xfffffffeu);
    __builtin_amdgcn_raw_buffer_store_b128(u32x4{d[0], d[1], d[2], d[3]}, rs, l16, h * 1024, 16);
    if (!hh) __builtin_amdgcn_raw_buffer_store_b64(u32x2{d[4], d[5]}, rs, l16, (32 + (h >> 2)) * 1024 + ((h >> 1) & 1) * 512 + (h & 1) * 8, 16);
  }
  SBAR();
  STAMP(2);
#pragma unroll
  for (int i = 0; i < nl; ++i) { z.wf[i] = ldl<bf16x8>(lds + wreg + l16 + i * 1024); }
  u32x4 of[10] = {};
  {
    const char* ob = ws + WS_OI + (size_t)(gmt * 40 + 10 * kq + 4) * 1024;
    unsigned pend = 0x3ff;
    for (int spin = 0; spin < (1 << 20); ++spin) {
#define K2_G(i, v, off) "s_bitcmp1_b32 %10, " #i "\n\ts_cbranch_scc0 1f\n\tglobal_load_dwordx4 %" #i ", %" #v ", %13 offset:" #off " sc1\n1:\n\t"
      asm volatile(K2_G(0, 11, -4096) K2_G(1, 11, -3072) K2_G(2, 11, -2048) K2_G(3, 11, -1024) K2_G(4, 11, 0) K2_G(5, 11, 1024) K2_G(6, 11, 2048) K2_G(7, 11, 3072)
                   K2_G(8, 12, 0) K2_G(9, 12, 1024) "s_waitcnt vmcnt(0)"
                   : "+v"(of[0]), "+v"(of[1]), "+v"(of[2]), "+v"(of[3]), "+v"(of[4]), "+v"(of[5]), "+v"(of[6]), "+v"(of[7]), "+v"(of[8]), "+v"(of[9])
                   : "s"(pend), "v"(l16), "v"(l16 + 4096), "s"(ob) : "scc", "memory");
      unsigned mx[10], all = 0;
#pragma unroll
      for (int i = 0; i < 10; ++i) {
        mx[i] = __builtin_elementwise_max(__builtin_elementwise_max(of[i][0], of[i][1]), __builtin_elementwise_max(of[i][2], of[i][3]));
        all = __builtin_elementwise_max(all, mx[i]);
      }
      if (__builtin_amdgcn_ballot_w64(all == ~0u) == 0) break;
      pend = 0;
#pragma unroll
      for (int i = 0; i < 10; ++i) pend |= __builtin_amdgcn_ballot_w64(mx[i] == ~0u) != 0 ? 1u << i : 0u;
    }
  }
  STAMP(4);
#pragma unroll
  for (int i = 0; i < 10; ++i)
#pragma unroll
    for (int u = 0; u < nu; ++u) z.acc[u] = MFMA16(z.wf[i * nu + u], BC(of[i]), z.acc[u]);
  {
    const lds_ptr rw = lds + wreg + r * (kh ? 272 : 400) + hh * 16;
#pragma unroll
    for (int u = 0; u < nu; ++u)
#pragma unroll
      for (int d = 0; d < 4; ++d) stl<f32x4>(rw + u * 128 + d * 32, f32x4{z.acc[u][4 * d], z.acc[u][4 * d + 1], z.acc[u][4 * d + 2], z.acc[u][4 * d + 3]});
  }
  LGKM_BAR();
  const lds_ptr rr[3] = {lds + K2_W0 + (t >> 4) * 400 + (t & 15) * 16, lds + K2_W1 + (t >> 4) * 272 + (t & 15) * 16, lds + K2_W0 + ((t & 255) >> 3) * 400 + 256 + (t & 7) * 16};
#pragma unroll
  for (int u = 0; u < 2 + kh; ++u) {
    f32x4 q = ldl<f32x4>(rr[u]);
#pragma unroll
    for (int s = 1; s < 4; ++s) q += ldl<f32x4>(rr[u] + s * (u == 1 ? 8704 : 21504));
    q[0] += blo(xv[u][0]) + blo(bv[u][0]); q[1] += bhi(xv[u][0]) + bhi(bv[u][0]);
    q[2] += blo(xv[u][1]) + blo(bv[u][1]); q[3] += bhi(xv[u][1]) + bhi(bv[u][1]);
    *(u32x2*)(out + 320 * nb + (u == 1 ? go[0] + 192 : go[u])) = u32x2{cvtpk(q[0], q[1]), cvtpk(q[2], q[3])};
  }
  PROF_END(1);
}
__global__ __launch_bounds__(512, 2) void k2(const char* __restrict__ x, const char* __restrict__ b2, char* __restrict__ ws, char* __restrict__ out) {
  const int w = __builtin_amdgcn_readfirstlane(threadIdx.x >> 6);
  if (w < 4) k2_body<0>(x, b2, ws, out, w); else k2_body<1>(x, b2, ws, out, w);
}

extern "C" void kernel_launch(void* const* d_in, const int* in_sizes, int n_in, void* d_out, int out_size, void* d_ws, size_t ws_size,
                              hipStream_t stream) {
  const char* x = (const char*)d_in[0]; const char* wqkv = (const char*)d_in[1]; const char* wo = (const char*)d_in[2];
  const char* w1 = (const char*)d_in[4]; const char* b1 = (const char*)d_in[5]; const char* w2 = (const char*)d_in[6]; const char* b2 = (const char*)d_in[7];
  char* ws = (char*)d_ws + ((ws_size - (40u  __H0__ )) & ~(size_t)0xfffff);
  (void)hipFuncSetAttribute((const void*)k1, hipFuncAttributeMaxDynamicSharedMemorySize, K1_LDS);
  (void)hipFuncSetAttribute((const void*)k2, hipFuncAttributeMaxDynamicSharedMemorySize, K2_LDS);
  k1<<<256, 768, K1_LDS, stream>>>(x, wqkv, w1, b1, w2, wo, ws);
  k2<<<256, 512, K2_LDS, stream>>>(x, b2, ws, (char*)d_out);
}
```

```cpp
#include <hip/hip_runtime.h>
#include <hip/hip_bf16.h>
#include <stdint.h>
#pragma clang diagnostic ignored "-Winline-asm"
#pragma clang diagnostic ignored "-Wint-to-pointer-cast"

using bf16x8 = __attribute__((ext_vector_type(8))) short;
using s16x4 = __attribute__((ext_vector_type(4))) short;
using f32x16 = __attribute__((ext_vector_type(16))) float;
using f32x4 = __attribute__((ext_vector_type(4))) float;
using f32x2 = __attribute__((ext_vector_type(2))) float;
using u32x16 = __attribute__((ext_vector_type(16))) unsigned;
using u32x8 = __attribute__((ext_vector_type(8))) unsigned;
using u32x4 = __attribute__((ext_vector_type(4))) unsigned;
using u32x2 = __attribute__((ext_vector_type(2))) unsigned;
typedef __attribute__((address_space(3))) char* lds_ptr;

#define DEVI __device__ __forceinline__
#define CE __host__ __device__ constexpr
#define SBAR() __builtin_amdgcn_sched_barrier(0)
#define PIN(x) asm volatile("" : "+v"(x))
#define MFMA16(a, b, c) __builtin_amdgcn_mfma_f32_32x32x16_bf16(a, b, c, 0, 0, 0)
#define MFMA8(a, b, c) __builtin_amdgcn_mfma_f32_32x32x8bf16_1k(a, b, c, 0, 0, 0)
#define MFMAS(a, b, c) __builtin_amdgcn_mfma_f32_16x16x32_bf16(a, b, c, 0, 0, 0)
#define WAIT_BAR(N) asm volatile("s_waitcnt vmcnt(%0) lgkmcnt(0)\n\ts_barrier" :: "n"(N) : "memory")
#define LGKM_BAR() asm volatile("s_waitcnt lgkmcnt(0)\n\ts_barrier" ::: "memory")
#define BC(p) __builtin_bit_cast(bf16x8, p)

constexpr int FF = 1728;
constexpr int QT = 1280;
constexpr int KVT = 2 * QT + 64;
constexpr int KVH = 66 * KVT;
constexpr unsigned WS_F0 = 1024, WS_F2 = 4096, WS_F1 = 36864;
constexpr unsigned WS_ST = 53248;
constexpr unsigned WS_QI = WS_ST + 2048 * 16;
constexpr unsigned WS_KVI = WS_QI + 32 * 64 * QT;
constexpr unsigned WS_HI = WS_KVI + 32 * KVH;
constexpr unsigned WS_W2I = WS_HI + 64 * 108 * 1024;
constexpr unsigned WS_OI = WS_W2I + 20 * 148 * 1024;
constexpr unsigned TAGC = 0x9E3779B9u;
constexpr unsigned DROP = 0x40000000u;
constexpr float LOG2E = 1.4426950408889634f;
constexpr float QSCALE = 0.22360679774997896f * LOG2E;
constexpr float GC1 = -2.f * 0.7978845608028654f * LOG2E, GC2 = GC1 * 0.044715f;

CE int recpos(int t) { const int a = t - 6, hi = a & 32, e = t < 6 ? 2 : hi ? -8 : 4; return 2 * (a & 24) + (hi >> 2) + (a & 7) + ((a & 24) == 24 ? e : 0); }
CE int hpos(int c) { const int i = 4 * (c >> 3) + (c & 3), b = (c >> 2) & 1, q = (i * (74 + 5 * b)) >> 10; return i + 13 * q + b * (14 + q); }
typedef __bf16 bf16x2_t __attribute__((ext_vector_type(2)));
DEVI unsigned cvtpk(float lo, float hi) { return __builtin_bit_cast(unsigned, __builtin_convertvector(f32x2{lo, hi}, bf16x2_t)); }
template <unsigned C = 0, bool DEV = false> DEVI void glds(const void* sbase, unsigned voff, unsigned lds_base) {
  if constexpr (DEV) asm volatile("s_add_u32 m0, %2, %3\n\ts_nop 0\n\tglobal_load_lds_dwordx4 %0, %1 sc1" :: "v"(voff), "s"(sbase), "s"(lds_base), "n"(C) : "m0", "scc", "memory");
  else asm volatile("s_add_u32 m0, %2, %3\n\ts_nop 0\n\tglobal_load_lds_dwordx4 %0, %1" :: "v"(voff), "s"(sbase), "s"(lds_base), "n"(C) : "m0", "scc", "memory");
}
DEVI void glds_m(const void* sbase, unsigned voff, unsigned lds_base, unsigned long long msk) {
  asm volatile("s_mov_b32 m0, %2\n\ts_mov_b64 exec, %3\n\tglobal_load_lds_dwordx4 %0, %1 sc1\n\ts_mov_b64 exec, -1" :: "v"(voff), "s"(sbase), "s"(lds_base), "s"(msk) : "m0", "memory");
}
DEVI s16x4 ldtr(lds_ptr p) { return __builtin_amdgcn_ds_read_tr16_b64_v4i16((__attribute__((address_space(3))) s16x4*)p); }
DEVI bf16x8 ldtr2(lds_ptr p, lds_ptr q) { s16x4 a = ldtr(p), b = ldtr(q); return __builtin_shufflevector(a, b, 0, 1, 2, 3, 4, 5, 6, 7); }
DEVI bf16x8 ldtr2(lds_ptr p, int off2) { return ldtr2(p, p + off2); }
template <typename T> DEVI T ldl(lds_ptr p) { return *(__attribute__((address_space(3))) T*)p; }
template <typename T> DEVI T ldg(const char* p, unsigned o) { return *(const __attribute__((address_space(1))) T*)((const __attribute__((address_space(1))) char*)p + o); }
template <typename T> DEVI void stg(char* p, unsigned o, T v) { *(__attribute__((address_space(1))) T*)((__attribute__((address_space(1))) char*)p + o) = v; }
template <typename T> DEVI void stl(lds_ptr p, T v) { *(__attribute__((address_space(3))) T*)p = v; }
typedef __amdgpu_buffer_rsrc_t rsrc_t;
DEVI rsrc_t mkrs(void* p) { return __builtin_amdgcn_make_buffer_rsrc(p, 0, 0x10000000, 0x00020000); }
DEVI void st16(rsrc_t rs, unsigned off, u32x4 v) { __builtin_amdgcn_raw_buffer_store_b128(v, rs, off, 0, 16); }
DEVI void st16(rsrc_t rs, unsigned off, unsigned so, u32x4 v) {
  __builtin_amdgcn_raw_buffer_store_b128(v, rs, off, so, 16);
  asm volatile("s_nop 1" :: "v"(v) : "memory");
}
DEVI void st8(rsrc_t rs, unsigned off, unsigned so, u32x2 v) { __builtin_amdgcn_raw_buffer_store_b64(v, rs, off, so, 16); }
DEVI void st4(rsrc_t rs, unsigned off, unsigned so, unsigned v) { __builtin_amdgcn_raw_buffer_store_b32(v, rs, off, so, 16); }
DEVI float blo(unsigned d) { return __uint_as_float(d << 16); }
DEVI float bhi(unsigned d) { return __uint_as_float(d & 0xffff0000u); }
DEVI u32x4 pack8(const float* v) { return u32x4{cvtpk(v[0], v[1]), cvtpk(v[2], v[3]), cvtpk(v[4], v[5]), cvtpk(v[6], v[7])}; }
constexpr int SPINS = 1 << 22;
DEVI void spin1(const char* p, unsigned voff, unsigned& f, unsigned tag) {
  unsigned n;
  asm volatile("s_mov_b32 %1, %5\n1:\n\tv_cmp_ne_u32 vcc, %4, %0\n\ts_cbranch_vccz 2f\n\ts_sleep 4\n\tglobal_load_dword %0, %2, %3 sc1\n\ts_waitcnt vmcnt(0)\n\t"
               "s_sub_u32 %1, %1, 1\n\ts_cbranch_scc0 1b\n2:" : "+v"(f), "=&s"(n) : "v"(voff), "s"(p), "s"(tag), "n"(SPINS) : "vcc", "scc", "memory");
}
DEVI unsigned tag_of(unsigned v) { unsigned t; asm volatile("v_readfirstlane_b32 %0, %1\n\ts_add_u32 %0, %0, %2" : "=s"(t) : "v"(v), "n"(TAGC) : "scc"); return t; }
constexpr int CH = 16 * KVT;
struct FS {
  u32x4 fl, hb[2];
  bf16x8 qm[2];
  s16x4 qr[2];
  float ga[8], k1, k2, sd;
  unsigned l16, r8, tag, no, w, h, m8, gmt, nb, rd, om, orr, fm, hl, hd[2], vp[4], kr, fo, f2, fz;
  const char *ws, *qp, *rb, *kv, *hp, *wp, *wo, *fp, *f1;
};
constexpr int KQX = 2;
#define SPIN(x) asm volatile("" : "+s"(x))
#define SET(d, expr) do { float v_ = (expr); PIN(v_); d = v_; } while (0)
#define SETU(d, expr) do { unsigned v_ = (expr); PIN(v_); d = v_; } while (0)
#define LM(m) __builtin_amdgcn_inverse_ballot_w64(m)
template <int i> DEVI void f_adr(FS& f) {
  const unsigned rt = f.w & 3, ch = f.w >> 2;
  if constexpr (i == 0) {
    unsigned bid = blockIdx.x; SPIN(bid);
    f.h = 4 * (bid & 7) + ((bid >> 3) & 3); f.m8 = (bid >> 2) & ~7u; f.gmt = f.m8 + (bid & 6) + ((bid >> 4) & 1); f.nb = 2 * (bid & 1) + ((bid >> 3) & 1);
    SPIN(f.h); SPIN(f.m8); SPIN(f.gmt); SPIN(f.nb); SPIN(f.w);
  }
  if constexpr (i == 1) { f.kv = f.ws + (WS_KVI + f.h * KVH); f.f1 = f.ws + (WS_F1 + f.h * 512); SPIN(f.kv); SPIN(f.f1); }
  if constexpr (i == 2) { f.hp = f.ws + (WS_HI + (f.gmt * 108 + 27 * rt) * 1024); SPIN(f.hp); }
  if constexpr (i == 3) { f.wo = f.ws + (WS_W2I + f.nb * 148 * 5120 + ch * 3072); SPIN(f.wo); }
  if constexpr (i == 4) { f.wp = f.wo + 27 * 5120 * rt; f.wo += (108 + 10 * (rt ^ KQX * ch)) * 5120; SPIN(f.wp); SPIN(f.wo); }
  if constexpr (i == 5) { f.qp = f.ws + (WS_QI + (f.h * 64 + f.m8 + 2 * rt) * QT); SPIN(f.qp); }
  if constexpr (i == 6) { f.rd = f.w * 4096; f.rb = f.kv + (54 * KVT + f.rd); f.fp = f.ws + (WS_F2 + f.gmt * 512); SPIN(f.rd); SPIN(f.rb); SPIN(f.fp); }
  if constexpr (i == 7) { f.r8 = 1024 + ((f.l16 >> 1) & 255); PIN(f.r8); }
  if constexpr (i == 8) { f.fm = f.m8 + 2 * rt + ch; f.om = WS_OI + f.fm * 40960; f.fm = WS_F2 + 128 + f.fm * 512 + f.h * 4; SPIN(f.om); SPIN(f.fm); }
  if constexpr (i == 9) { f.om += (f.h >> 2) * 5120; f.orr = f.om + 3072 + (f.h & 3) * 256; f.om += (f.h & 3) * 1024; SPIN(f.om); SPIN(f.orr); }
  if constexpr (i == 0) SETU(f.vp[1], (f.l16 >> 2 & 48) | (f.l16 >> 3 & 64));
  if constexpr (i == 1) SETU(f.vp[0], f.vp[1] | (f.l16 & 32) << 2 | QT);
  if constexpr (i == 2) { SETU(f.vp[0], f.vp[0] | (f.l16 >> 1 & 8)); SETU(f.vp[1], QT + 1024 + (f.vp[1] >> 1)); }
  if constexpr (i == 3) { SETU(f.vp[0], LM(0x1111000011110000ull) ? f.vp[1] : f.vp[0]); SETU(f.vp[3], LM(0x1111000011110000ull) ? 64 : 256); }
  if constexpr (i == 4) SETU(f.vp[1], 2 * QT | (f.l16 >> 2 & 8));
  if constexpr (i == 5) { SETU(f.vp[0], LM(0xeeee0000eeee0000ull) ? f.vp[1] : f.vp[0]); SETU(f.vp[3], LM(0xeeee0000eeee0000ull) ? 0 : f.vp[3]); }
  if constexpr (i == 6) { SETU(f.vp[1], f.vp[0] + f.vp[3]); SETU(f.vp[2], f.vp[1] + f.vp[3]); SETU(f.vp[3], f.vp[2] + f.vp[3]); }
  if constexpr (i == 8) { SETU(f.kr, LM(0xffffffffull) ? f.r8 : 2 * QT); SETU(f.fz, WS_F0 | (f.l16 >> 2 & 60)); }
  if constexpr (i == 9) { SETU(f.fo, ((14 * rt) >> 2) * 8 + (f.l16 >> 2 & 28)); SETU(f.f2, LM(1ull) ? f.f2 : DROP); f.qr[0] = f.qr[1] = s16x4{}; PIN(f.qr[0]); PIN(f.qr[1]); }
  SBAR();
}
DEVI void o_st(const FS& f, rsrc_t rs, u32x4 a, u32x2 b) { st16(rs, f.l16, f.om, a); st8(rs, f.r8 | (f.l16 & 512) << 21, f.orr, b); }
DEVI void o_mark(const FS& f, rsrc_t rs) { u32x4 e = {~0u, ~0u, ~0u, ~0u}; PIN(e); o_st(f, rs, e, u32x2{e[0], e[1]}); SBAR(); }
DEVI unsigned differ(u32x4 v, unsigned t) { return (v[0] ^ t) | (v[1] ^ t) | (v[2] ^ t) | (v[3] ^ t); }
DEVI unsigned missing(const FS& f) { return (unsigned)__builtin_amdgcn_ballot_w64(differ(f.fl, f.tag) != 0) & 0xffffff; }
DEVI void f_open(FS& f) {
  asm volatile("s_mov_b64 exec, 0xffffff\n\tglobal_load_dwordx4 %0, %1, %2 sc1\n\ts_mov_b64 exec, -1" : "=v"(f.fl) : "v"(f.l16), "s"(f.f1) : "memory"); SBAR();
}
template <int VM> DEVI void f_see(FS& f) { asm volatile("s_waitcnt vmcnt(%1)" : "+v"(f.fl) : "n"(VM) : "memory"); f.no = missing(f); SPIN(f.no); SBAR(); }
template <int i> DEVI void f_ld(FS& f) {
  if constexpr (i < 4) asm volatile("s_mov_b32 m0, %2\n\ts_nop 0\n\tglobal_load_lds_dwordx4 %0, %1 offset:%3 sc1" :: "v"(f.l16), "s"(f.rb), "s"(f.rd), "n"(i * 1024) : "m0", "memory");
  else if constexpr (i < 6) asm volatile("global_load_dwordx4 %0, %1, %2 offset:%3 sc1" : "=v"(f.qm[i & 1]) : "v"(f.l16), "s"(f.qp), "n"((i & 1) * QT) : "memory");
  else asm volatile("s_mov_b32 exec_hi, 0\n\tglobal_load_dwordx2 %0, %1, %2 offset:%3 sc1\n\ts_mov_b32 exec_hi, -1" : "+v"(f.qr[i & 1]) : "v"(f.r8), "s"(f.qp), "n"((i & 1) * QT) : "memory");
  SBAR();
}
template <int VM> DEVI void f_got(FS& f) { asm volatile("s_waitcnt vmcnt(%4)" : "+v"(f.qm[0]), "+v"(f.qr[0]), "+v"(f.qm[1]), "+v"(f.qr[1]) : "n"(VM) : "memory"); SBAR(); }
constexpr int BSLEEP = 4;
template <int SLEEP> DEVI void f_wait(FS& f) {
#pragma unroll 1
  for (int i = 0; i < SPINS; ++i) {
    f_open(f); f_see<0>(f);
    if (!f.no) break;
    __builtin_amdgcn_s_sleep(SLEEP);
  }
}
DEVI void f_all(FS& f) { f_ld<0>(f); f_ld<1>(f); f_ld<2>(f); f_ld<3>(f); f_ld<4>(f); f_ld<5>(f); f_ld<6>(f); f_ld<7>(f); }
struct ST { u32x4 a; unsigned b, ad; float s[4]; };
DEVI void st_ld(ST& s, const char* row, unsigned l16) {
  asm volatile("global_load_dwordx4 %0, %2, %3\n\tv_lshrrev_b32 %1, 2, %2\n\tglobal_load_dword %1, %1, %3 offset:1024" : "=&v"(s.a), "=&v"(s.b) : "v"(l16), "s"(row) : "memory");
}
template <int i> DEVI void st_op(ST& s, unsigned hi) {
  float t, u;
  if constexpr (i == 0) asm volatile("v_lshlrev_b32 %0, 16, %4\n\tv_and_b32 %1, %5, %4\n\tv_mul_f32 %2, %0, %0\n\tv_mul_f32 %3, %1, %1" : "=&v"(s.s[0]), "=&v"(s.s[1]), "=&v"(s.s[2]), "=&v"(s.s[3]) : "v"(s.b), "s"(hi));
  if constexpr (i > 0 && i < 5)
    asm volatile("v_lshlrev_b32 %4, 16, %6\n\tv_and_b32 %5, %7, %6\n\tv_add_f32 %0, %0, %4\n\tv_add_f32 %1, %1, %5\n\tv_fmac_f32 %2, %4, %4\n\tv_fmac_f32 %3, %5, %5"
                 : "+v"(s.s[0]), "+v"(s.s[1]), "+v"(s.s[2]), "+v"(s.s[3]), "=&v"(t), "=&v"(u) : "v"(s.a[(i - 1) & 3]), "s"(hi));
  if constexpr (i == 5) asm volatile("v_add_f32 %0, %0, %2\n\tv_add_f32 %1, %1, %3" : "+v"(s.s[0]), "+v"(s.s[2]) : "v"(s.s[1]), "v"(s.s[3]));
  if constexpr (i == 6) asm volatile("v_permlane32_swap_b32 %0, %1" : "+v"(s.s[0]), "+v"(s.s[2]));
  if constexpr (i == 7) asm volatile("v_add_f32 %0, %0, %1" : "+v"(s.s[0]) : "v"(s.s[2]));
  if constexpr (i > 7 && i < 12) asm volatile("v_add_f32_dpp %0, %0, %0 row_ror:%1 row_mask:0xf bank_mask:0xf" : "+v"(s.s[0]) : "n"(1 << (i & 3)));
  if constexpr (i == 12) asm volatile("v_add_f32_dpp %0, %0, %0 row_bcast:15 row_mask:0xa bank_mask:0xf" : "+v"(s.s[0]));
  SBAR();
}
template <int i = 0> DEVI void st_all(ST& s) { st_op<i>(s, 0xffff0000u); if constexpr (i > 3) asm volatile("s_nop 1"); if constexpr (i < 12) st_all<i + 1>(s); }

constexpr int NP = 12, QRATE = 8, GRATE = 4, GB = 46, GR = 56, GL = 72, KFL = 98, KFS = 5, KLS = 2, HS = 119, R2 = 40, PFG = 74, SL = 24, SB = 34, PQG = -1, PQH = 8, PMG = 4, QR0 = 192;
constexpr int GE = 79, GF = 118, SS = 9;
constexpr int MOFF[20] = {10, 11, 12, 13, 14, 0, 1, 2, 3, 15, 5, 16, 17, 18, 19, 10, 11, 12, 13, 14}, MAV[15] = {22, 22, 26, 26, 30, 22, 22, 26, 26, 30, 30, 34, 34, 38, 38};
constexpr unsigned CS = 2 * 16384;
constexpr int Q0 = 42, QN = 4 * 16, G0P = GE + 1, GO = 16, GN = GO + 2 * 60;
struct Sch { int key[60]; };
CE Sch mk_sch() {
  Sch s{};
  int h = 0;
  for (; h < NP; ++h) s.key[h] = h < 4 ? h - 100 : 2 * h - 80;
  for (int G = 0; G < 120 && h < 60; ++G)
    for (int i = 0; i < (((G & 3) == 1 && G < R2) || (G >= 38 && G < GR) ? 2 : G < GR || !(G & 1)); ++i) {
      const int av = h < 20 ? 0 : h < 40 ? 4 * ((h >> 2) - 5) + 2 : h < 55 ? MAV[h - 40] : GR;
      if (h < 60 && av <= G) s.key[h++] = 8 * G + 1 + i;
    }
  return s;
}
constexpr Sch SCH = mk_sch();
CE int fill_at(int key) { for (int h = 0; h < 60; ++h) if (SCH.key[h] == key) return h; return -1; }
CE int imax(int a, int b) { return a > b ? a : b; }
CE int q_st(int t) { return imax(Q0 + (16 * t + 15) / QRATE, (SCH.key[54] >> 3) + (t >> 1)); }
CE int g_st(int t) { return imax(G0P + (GO + 60 * t + 59) / GRATE, HS); }
constexpr int PQK = PQG < 0 ? 2 * PQH - 81 : 8 * PQG + 3, PMK = PMG < 0 ? -40 : 8 * PMG + 3;
CE int landed(int key) { int G = 2; while (SCH.key[G + 5] < key) G += 4; return G; }
CE int vm_in(int a, int b) {
  const int o[19] = {-92, -91, -90, -89, PQK, PMK, 8 * PFG + 4, 8 * PFG + 4, 8 * SS + 3, 8 * SL + 3, 8 * q_st(0) + 3, 8 * q_st(1) + 3, 8 * q_st(2) + 3, 8 * q_st(3) + 3, 8 * q_st(3) + 3, 8 * GL, 8 * KFL + 4, 8 * g_st(0) + 5, 8 * g_st(1) + 5};
  int n = 0;
  for (int i = 0; i < 19; ++i) n += o[i] > a && o[i] < b;
  for (int i = 0; i < 8; ++i) n += 8 * (KFL + KFS + KLS * i) + 4 > a && 8 * (KFL + KFS + KLS * i) + 4 < b;
  for (int h = 0; h < 60; ++h) n += SCH.key[h] > a && SCH.key[h] < b;
  return n;
}
CE bool is_bar(int G) { return G < 40 ? (G & 3) == 2 : G == GB || G == GR || G == GL || G == GE || G == GF; }
CE int bar_vm(int G) {
  int e = G + 1;
  while (e < 120 && !is_bar(e)) ++e;
  const int h = G < 38 ? G + 5 : 40 + (((e - 37) >> 2) < 19 ? (e - 37) >> 2 : 19);
  return G == GF ? 63 : vm_in(imax(G == GL ? 8 * q_st(3) + 3 : G == SB ? 8 * SL + 3 : -100, SCH.key[h]), 8 * G);
}
CE bool sch_ok() {
  for (int j = 0; j < 20; ++j) { int b = imax(36 + 4 * j, 38); while (!is_bar(b)) --b; if (SCH.key[40 + j] >= 8 * b) return false; }
  return Q0 + QN / QRATE <= 51 && GL > 55 && GR >= 56 && GL <= GE && q_st(3) < 60 && G0P + GN / GRATE < GF && NP >= 8 && NP <= 20 && KFL > GE && KFL + KFS + 7 * KLS < 120 && is_bar(SB) && SB < 38 && SL < SB && PFG > 70 && PFG < KFL && PQG < 20 && PMG < 20 && QR0 % 64 == 0;
}
static_assert(sch_ok(), "schedule");
struct Tab { unsigned a[30][32], b[3][64][8]; };
CE Tab mk_tab() {
  Tab T{};
  for (unsigned e = 0; e < 30; ++e) {
    const unsigned n = e >> 1, ch = e & 1, n5 = n % 5, ps = n < 10 ? 512 : 128, hb = n < 5 ? 64 * QT : KVH;
    unsigned* a = T.a[e];
    for (unsigned t = 0; t < 4; ++t) {
      for (unsigned i = 0; i < 2; ++i) {
        const unsigned o = 16 * n5 + 8 * ch + 2 * t + i, c = o % 5;
        a[2 * t + i] = c == 2 ? ~0u : 0u;
        a[8 + 2 * t + i] = 2 * (o / 5) * hb + (c == 2 ? 0u : c * ps + (c > 2 ? hb - 3 * ps : 0u)) - (i ? a[8 + 2 * t] : 0u);
      }
      a[16 + t] = WS_HI + hpos(8 * (n < 13 ? n : 13) + 4 * (t >> 1) + 2 * ch + (t & 1)) * 1024;
    }
    a[20] = 4 * e; a[23] = 64 * e;
    a[21] = n < 5 ? DROP : QT + (6 * n5 + (n5 == 3) + 4 * ch) * KVH;
    a[22] = 128 * n5; a[24] = n < 14 ? 0 : DROP; a[25] = n == 13 ? DROP : 0;
    a[26] = n < 10 ? 128 : 256; a[29] = hb;
    a[30] = n < 14 ? 256 * n : 0; a[31] = n < 13 ? 256 * n + 128 : 0;
  }
  for (int k = 0; k < 3; ++k)
    for (int mt = 0; mt < 64; ++mt) {
      unsigned* b = T.b[k][mt];
      b[0] = k == 0 ? WS_QI + mt * QT : k == 1 ? WS_KVI + recpos(mt) * KVT : WS_KVI + QT + recpos(mt + 1) * KVT;
      b[1] = k < 2 ? b[0] : WS_KVI + QT + recpos(mt + 2) * KVT;
      b[2] = k < 2 ? b[1] + QT : b[1]; b[3] = mt * 108 * 1024;
      b[4] = WS_F2 + mt * 512; b[5] = k ? 0x3f800000u : __builtin_bit_cast(unsigned, QSCALE); b[6] = k * 128 + (mt >> 2) * 8; b[7] = WS_ST - 256 + (mt >> 2) * 2048 - (mt & 3) * 496;
    }
  return T;
}
__constant__ static const Tab TAB = mk_tab();
struct P1 {
  bf16x8 xf[40], wf[4], sel;
  f32x16 aq[2], am[2];
  f32x4 cs[2], cb, tc, td;
  u32x4 ap, bp, pk[2], qv[4];
  f32x2 t, u;
  u32x2 sv;
  ST st;
  float r, sd, rq, k1, k2;
  unsigned xa[4], wa[4], qa[4], vx, vq, vm, bias, b2, tg, lm, lr, ad, sa, ca, cw, hm, hl, fa, kd, tid, pf[2], pe;
  const char *xb[2], *qb, *mb[2], *sp, *pq, *pw;
  unsigned ldst, qo[4][2], hd[2][2], hx, n5, fs, sc, wx, hi, pm, so;
  unsigned long long rm[4], odd, low;
  int w;
  rsrc_t rs;
};
CE int p_off(int h) { return h >= 40 ? 0 : h & 2 ? (h & 1) * 128 : h == 20 ? 0 : (h >> 2) * 128; }
template <int h> DEVI void p_m0(P1& z) {
  if constexpr (h >= 0)
    asm volatile("s_add_u32 m0, %0, %1" :: "s"(z.ldst), "n"((h >= 40 ? MOFF[h - 40] : (h & 2) * 5 + (h >> 2) % 5 * 2 + (h & 1)) * 8192 - p_off(h)) : "m0", "scc");
}
template <int h> DEVI void p_ld(P1& z) {
  if constexpr (h >= 0) {
    if constexpr (h > 3 && h < 40 && (h & 3) == 2) z.qb += 64 * 3840;
    asm volatile("global_load_lds_dwordx4 %0, %1 offset:%2" :: "v"(h >= 40 ? z.vm : h & 2 ? z.vq : z.vx),
                 "s"(h >= 40 ? z.mb[h / 50] + h % 10 * 64 * 3456 : h & 2 ? z.qb : z.xb[h & 1] + (h == 20 ? 640 : 0)), "n"(p_off(h)) : "memory");
    SBAR();
  }
}
template <int A, int B> DEVI void p_lds(P1& z) { if constexpr (A < B) { p_m0<A>(z); if constexpr (A < 4 || (A & 3) != 2) asm volatile("s_nop 0"); p_ld<A>(z); p_lds<A + 1, B>(z); } }
DEVI void l2_q(P1& z) { asm volatile("s_mov_b64 exec, %2\n\tglobal_load_dword %0, %0, %1\n\ts_mov_b64 exec, -1" : "+v"(z.pf[0]) : "s"(z.pq), "n"((1 << (640 - QR0) / 32) - 1) : "memory"); SBAR(); }
DEVI void l2_m(P1& z) { asm volatile("s_mov_b64 exec, %2\n\tglobal_load_dword %0, %0, %1\n\ts_mov_b64 exec, -1" : "+v"(z.pf[1]) : "s"(z.pw), "s"((unsigned long long)z.pe) : "memory"); SBAR(); }
template <int D> DEVI void bump(unsigned& a) { if constexpr (D != 0) asm volatile("v_add_u32 %0, %1, %0" : "+v"(a) : "n"(D)); }
CE int xwin(int i) { return i == 4 || i > 7; }
template <int G, bool RE = false> DEVI void p_ldx(P1& z) {
  constexpr int i = G >> 2;
  if constexpr (G < 40) {
    if constexpr (!RE && i > 0) bump<(xwin(i) - xwin(i - 1)) * 16384>(z.xa[G & 3]);
    z.xf[G] = ldl<bf16x8>((lds_ptr)z.xa[G & 3] + (i % 5 - xwin(i)) * 16384); SBAR();
  }
}
CE int mwin(int j) { return MOFF[j] < 8 ? 0 : MOFF[j] < 16 ? 10 : 12; }
template <int K> DEVI void b_ldw(P1& z) {
  if constexpr (K < 80) {
    constexpr int j = K >> 2;
    if constexpr (K < 4) asm volatile("v_add_u32 %0, %1, %0" : "+v"(z.wa[K]) : "s"(z.wx));
    else bump<(mwin(j) - mwin(j - 1)) * 8192>(z.wa[K & 3]);
    z.wf[K & 3] = ldtr2((lds_ptr)z.wa[K & 3] + (MOFF[j] - mwin(j)) * 8192, 256); SBAR();
  }
}
template <int G, int t> DEVI void a_ldw(P1& z) {
  if constexpr (G < 40) {
    constexpr int i = G >> 2;
    if constexpr (t == 0 && i > 0) bump<(xwin(i) - xwin(i - 1)) * 16384>(z.wa[G & 3]);
    z.wf[2 * (G & 1) + t] = ldtr2((lds_ptr)z.wa[G & 3] + (i % 5 - xwin(i)) * 16384 + t * 512, 256); SBAR();
  } else b_ldw<2 * (G - 40) + t>(z);
}
template <int g> DEVI void a_stat(P1& z, FS& f) {
  if constexpr (g >= 5 && g < 18) st_op<g - 5>(z.st, z.hi);
  if constexpr (g == 12) SETU(z.ad, LM(0x0001000000010000ull) ? z.sa : DROP);
  if constexpr (g == 2 * SS + 1) st8(z.rs, z.ad, z.so, u32x2{__float_as_uint(z.st.s[0]), f.tag});
  if constexpr (g == 2 * SL + 1) asm volatile("global_load_dwordx2 %0, %1, %2 sc1" : "=v"(z.sv) : "v"(z.sa), "s"(z.sp) : "memory");
  if constexpr (g == 2 * SB) {
    unsigned long long t; unsigned n;
    asm volatile("s_mov_b32 %2, %6\n1:\n\tv_lshrrev_b64 %1, 32, %0\n\tv_cmp_ne_u64 vcc, %5, %1\n\ts_cbranch_vccz 2f\n\ts_sleep 2\n\tglobal_load_dwordx2 %0, %3, %4 sc1\n\ts_waitcnt vmcnt(0)\n\t"
                 "s_sub_u32 %2, %2, 1\n\ts_cbranch_scc0 1b\n2:" : "+v"(z.sv), "=&v"(t), "=&s"(n) : "v"(z.sa), "s"(z.sp), "s"((unsigned long long)f.tag), "n"(SPINS) : "vcc", "scc", "memory");
  }
  if constexpr (g == 2 * SB + 1) SETU(z.sv[1], z.sv[0]);
  if constexpr (g == 2 * SB + 2) { unsigned a = z.sv[0], b = z.sv[1]; asm volatile("v_permlane32_swap_b32 %0, %1" : "+v"(a), "+v"(b)); z.u = f32x2{__uint_as_float(a), __uint_as_float(b)}; }
  if constexpr (g == 2 * SB + 3)
    asm volatile("v_mul_f32 %0, 0xbacccccd, %0\n\tv_mul_f32 %1, 0x3acccccd, %1\n\tv_fma_f32 %1, -%0, %0, %1\n\tv_max_f32 %1, 0, %1\n\tv_add_f32 %1, 0x3727c5ac, %1\n\tv_rsq_f32 %2, %1"
                 : "+v"(z.u[0]), "+v"(z.u[1]), "=v"(z.r));
  if constexpr (g == 2 * SB + 4) {
    unsigned t, b0, b1, b2;
    asm volatile("v_mul_f32 %0, %7, %8\n\tv_cndmask_b32 %6, %0, %6, %9\n\tv_and_b32 %7, %10, %6\n\tv_sub_f32 %7, %6, %7\n\tv_perm_b32 %3, %7, %6, %11\n\tv_perm_b32 %5, %7, %6, %11\n\t"
                 "v_and_b32 %2, %10, %7\n\tv_sub_f32 %7, %7, %2\n\tv_perm_b32 %4, %7, %6, %11\n\tv_mul_f32 %1, %12, %8"
                 : "=&v"(z.sd), "=&v"(z.rq), "=&v"(t), "=&v"(b0), "=&v"(b1), "=&v"(b2), "+v"(z.u[0]), "+v"(z.u[1]) : "v"(z.r), "s"(z.low), "s"(z.hi), "s"(z.pm), "s"(z.sc));
    z.bp[0] = b0; z.bp[1] = b1; z.bp[2] = b2;
  }
  SBAR();
}
DEVI void a_split(P1& z, const f32x4& c, unsigned b) {
  unsigned t, u, a0, a1, a2;
  asm volatile("v_add_f32 %0, %5, %6\n\tv_add_f32 %1, %7, %8\n\tv_add_f32 %0, %0, %1\n\tv_cndmask_b32 %0, %9, %0, %10\n\tv_and_b32 %1, %11, %0\n\tv_perm_b32 %2, %0, %0, %12\n\t"
               "v_sub_f32 %1, %0, %1\n\tv_perm_b32 %3, %0, %1, %12\n\tv_and_b32 %0, %11, %1\n\tv_sub_f32 %0, %1, %0\n\tv_perm_b32 %4, %1, %0, %12"
               : "=&v"(t), "=&v"(u), "=&v"(a0), "=&v"(a1), "=&v"(a2) : "v"(c[0]), "v"(c[1]), "v"(c[2]), "v"(c[3]), "v"(b), "s"(z.low), "s"(z.hi), "s"(z.pm));
  z.ap[0] = a0; z.ap[1] = a1; z.ap[2] = a2; SBAR();
}
template <int G> DEVI void a_step(P1& z, FS& f) {
  constexpr int p = G & 3, q = 2 * (G & 1);
  const bf16x8 w0 = z.wf[q], w1 = z.wf[q + 1];
  z.aq[0] = MFMA16(w0, z.xf[G], G ? z.aq[0] : f32x16{}); SBAR();
  if constexpr (p == 2) {
    WAIT_BAR(bar_vm(G));
    z.aq[1] = MFMA16(w1, z.xf[G], z.aq[1]); SBAR();
    if constexpr (G == 2) f.tag = tag_of(z.tg);
    if constexpr (G == landed(PQK)) asm volatile("" :: "v"(z.pf[0]));
    if constexpr (G == landed(PMK)) asm volatile("" :: "v"(z.pf[1]));
    if constexpr (G == 38) { z.tc = ldl<f32x4>((lds_ptr)z.ca); z.td = ldl<f32x4>((lds_ptr)z.ca + 512); SBAR(); }
  }
  p_m0<fill_at(8 * G + 1)>(z);
  a_ldw<G + 2, 0>(z);
  if constexpr (p == 1) a_ldw<G + 2, 1>(z); else p_ldx<G + (p ? 2 : 3)>(z);
  p_ld<fill_at(8 * G + 1)>(z);
  p_m0<fill_at(8 * G + 2)>(z);
  if constexpr (p == 2) a_ldw<G + 2, 1>(z);
  a_stat<2 * G>(z, f);
  if constexpr (G < 36 ? p == 1 : p == 0) { z.cs[0] = MFMAS(z.sel, w0, G > 1 ? z.cs[0] : f32x4{}); SBAR(); }
  if constexpr (p != 2) { z.aq[1] = MFMA16(w1, z.xf[G], G ? z.aq[1] : f32x16{}); SBAR(); }
  if constexpr (p == 0 || p == 3) a_ldw<G + 2, 1>(z);
  if constexpr (p == 3) p_ldx<G + 3>(z);
  p_ld<fill_at(8 * G + 2)>(z);
  a_stat<2 * G + 1>(z, f);
  if constexpr (G == PQG) l2_q(z);
  if constexpr (G == PMG) l2_m(z);
  if constexpr (G < 36 ? p == 3 : p == 0) { z.cs[1] = MFMAS(z.sel, w1, G > 3 ? z.cs[1] : f32x4{}); SBAR(); }
  if constexpr (G == 37) { stl<float>((lds_ptr)z.cw, z.cs[0][0]); stl<float>((lds_ptr)z.cw + 512, z.cs[1][0]); SBAR(); }
  if constexpr (G == 38) { SETU(z.ap[3], 0); SETU(z.bp[3], 0); SBAR(); }
  if constexpr (G == 39) a_split(z, z.tc, z.ap[3]);
  if constexpr (G < 39) a_step<G + 1>(z, f);
}
template <int U> DEVI void q_op(P1& z) {
  constexpr int t = U >> 4, k = U & 15, e = 8 * (t & 1) + 2 * (k / 3);
  f32x16& a = z.aq[t >> 1];
  u32x4& pk = z.pk[0];
  if constexpr (k < 12 && k % 3 < 2) SET(a[e + k % 3], a[e + k % 3] * z.rq);
  if constexpr (k < 12 && k % 3 == 2 && k > 2) SETU(pk[k / 3 - 1], cvtpk(a[e - 2], a[e - 1]));
  if constexpr (k == 2) SETU(z.ad, LM(z.rm[t]) ? z.lr : z.lm);
  if constexpr (k == 12) SETU(z.fa, z.hm & z.qo[t][1]);
  if constexpr (k == 13) SETU(pk[3], cvtpk(a[e - 2], a[e - 1]));
  if constexpr (k == 14) SETU(z.qa[t], z.ad + z.fa + z.qo[t][0]);
  if constexpr (k == 15) {
#define DPPX(d, a, b) "v_cndmask_b32_dpp " d ", " a ", " b ", vcc quad_perm:[1,0,3,2] row_mask:0xf bank_mask:0xf\n\t"
    unsigned o0, o1, o2, o3;
    asm volatile("s_orn2_b64 vcc, %9, %8\n\t" DPPX("%2", "%4", "%6") DPPX("%3", "%5", "%7") "s_nand_b64 vcc, %9, %8\n\t" DPPX("%0", "%6", "%4") DPPX("%1", "%7", "%5")
                 : "=&v"(o0), "=&v"(o1), "=&v"(o2), "=&v"(o3) : "v"(pk[0]), "v"(pk[1]), "v"(pk[2]), "v"(pk[3]), "s"(z.rm[t]), "s"(z.odd) : "vcc", "scc");
    z.qv[t] = u32x4{o0, o1, o2, o3};
  }
  SBAR();
}
template <int k, typename Z> DEVI void g_one(Z& z, f32x2& t, u32x4& pk, float a) {
  constexpr int o = (k % 15) >> 1, i = (k % 15) & 1;
  if constexpr (o == 0) SET(t[i], a * a);
  if constexpr (o == 1) SET(t[i], __builtin_fmaf(z.k2, t[i], z.k1));
  if constexpr (o == 2) SET(t[i], t[i] * a);
  if constexpr (o == 3) SET(t[i], __builtin_amdgcn_exp2f(t[i]));
  if constexpr (o == 4) SET(t[i], __builtin_fmaf(t[i], z.sd, z.sd));
  if constexpr (o == 5) SET(t[i], __builtin_amdgcn_rcpf(t[i]));
  if constexpr (o == 6) SET(t[i], t[i] * a);
  if constexpr (o == 7) SETU(pk[k / 15], cvtpk(t[0], t[1]));
  SBAR();
}
template <int A, int B> DEVI void g_fin(FS& f, f32x2& t) { if constexpr (A < B) { g_one<A>(f, t, f.hb[1], f.ga[A / 15 * 2 + (A % 15 & 1)]); g_fin<A + 1, B>(f, t); } }
template <int hf, int U> DEVI void g_op(P1& z) {
  f32x16& a = z.am[hf];
  if constexpr (U == 0) a_split(z, z.tc, hf ? z.b2 : z.bias);
  if constexpr (U == 8) { if constexpr (hf) asm volatile("s_nop 1"); a = MFMA16(BC(z.ap), BC(z.bp), a); }
  if constexpr (U >= GO) {
    constexpr int t = (U - GO) / 60, k = (U - GO) % 60;
    g_one<k>(z, z.t, z.pk[t], a[8 * t + k / 15 * 2 + (k % 15 & 1)]);
  }
  SBAR();
}
template <int A, int B> DEVI void q_ops(P1& z) { if constexpr (A < B && A < QN) { q_op<A>(z); q_ops<A + 1, B>(z); } }
template <int hf, int A, int B> DEVI void g_ops(P1& z) { if constexpr (A < B && A < GN) { g_op<hf, A>(z); g_ops<hf, A + 1, B>(z); } }
template <int G> DEVI void b_step(P1& z, FS& f) {
  constexpr int K = G - 40, hf = K / 40, k = K % 40;
  z.am[hf] = MFMA16(z.wf[K & 3], z.xf[k], k ? z.am[hf] : f32x16{}); SBAR();
  if constexpr (is_bar(G)) WAIT_BAR(bar_vm(G));
  if constexpr (!(K & 3)) { z.cb = MFMAS(z.sel, z.wf[0], k ? z.cb : f32x4{}); SBAR(); }
  if constexpr (G == GL) { st4(z.rs, z.fa, z.fs, f.tag); SBAR(); }
  if constexpr (G == GE || G == GF) { z.tc = ldl<f32x4>((lds_ptr)z.ca + 2048 + hf * 512); SBAR(); }
  p_m0<fill_at(8 * G + 1)>(z);
  b_ldw<K + 4>(z);
  p_ld<fill_at(8 * G + 1)>(z);
  p_m0<fill_at(8 * G + 2)>(z);
  p_ld<fill_at(8 * G + 2)>(z);
  if constexpr (k >= 29 && k < 37) p_ldx<k + 3, true>(z);
  if constexpr (k == 37) { stl<float>((lds_ptr)z.cw + 2048 + hf * 512, z.cb[0]); SBAR(); }
  if constexpr (G == 40) { z.aq[0] = MFMA16(BC(z.ap), BC(z.bp), z.aq[0]); SBAR(); }
  if constexpr (G == 41) a_split(z, z.td, z.ap[3]);
  if constexpr (G == 42) { z.aq[1] = MFMA16(BC(z.ap), BC(z.bp), z.aq[1]); SBAR(); }
  if constexpr (G >= Q0) q_ops<(G - Q0) * QRATE, (G - Q0 + 1) * QRATE>(z);
  if constexpr (G == q_st(0)) st16(z.rs, z.qa[0], z.qv[0]);
  if constexpr (G == q_st(1)) st16(z.rs, z.qa[1], z.qv[1]);
  if constexpr (G == q_st(2)) st16(z.rs, z.qa[2], z.qv[2]);
  if constexpr (G == q_st(3)) { st16(z.rs, z.qa[3], z.qv[3]); st16(z.rs, z.kd, u32x4{0x3f80u, 0u, 0u, 0u}); }
  if constexpr (G >= G0P) g_ops<0, (G - G0P) * GRATE, (G - G0P + 1) * GRATE>(z);
  if constexpr (G == 51) { SET(z.k1, z.r * GC1); SET(z.k2, z.r * z.r); SET(z.rq, z.r * GC2); SETU(z.ad, 10 * z.tid - z.n5); }
  if constexpr (G == 52) { SET(z.k2, z.k2 * z.rq); SETU(z.ad, z.ad + 9 * (z.tid & 1)); }
  if constexpr (G == 53) SETU(z.fa, WS_F1 + (z.tid >> 1) * 512 + (z.tid & 1) * 4);
  if constexpr (G == 54) SETU(z.fa, z.ad < 128 ? z.fa : DROP);
  if constexpr (G == 55) {
    unsigned b; asm volatile("v_lshlrev_b32 %0, 16, %1" : "=v"(b) : "v"(z.bias));
    auto s = __builtin_amdgcn_permlane32_swap(b, b, false, false);
    SETU(z.bias, s[0]); SETU(z.b2, s[1]);
  }
  SBAR();
  if constexpr (G >= 61 && G < 71) f_adr<G - 61>(f);
  if constexpr (G == PFG) o_mark(f, z.rs);
  if constexpr (G == KFL) f_open(f);
  if constexpr (G == KFL + KFS) f_see<vm_in(8 * KFL + 4, 8 * G + 4)>(f);
  if constexpr (G >= KFL + KFS && (G - KFL - KFS) % KLS == 0 && G < KFL + KFS + 8 * KLS) f_ld<(G - KFL - KFS) / KLS>(f);
  if constexpr (G == g_st(0)) st16(z.rs, z.hl, z.hd[0][0], z.pk[0]);
  if constexpr (G == g_st(1)) st16(z.rs, z.hl, z.hd[0][1], z.pk[1]);
  if constexpr (G < 119) b_step<G + 1>(z, f);
}

constexpr unsigned WE0 = 2 * CH, WE1 = WE0 + 4 * 10240, WL0 = 24576, RD2 = 65536, LDS_BYTES = 163840;

DEVI float max16(const f32x16& s) {
  float a = __builtin_fmaxf(__builtin_fmaxf(s[0], s[1]), s[2]), b = __builtin_fmaxf(__builtin_fmaxf(s[3], s[4]), s[5]);
  a = __builtin_fmaxf(__builtin_fmaxf(a, s[6]), s[7]); b = __builtin_fmaxf(__builtin_fmaxf(b, s[8]), s[9]);
  a = __builtin_fmaxf(__builtin_fmaxf(a, s[10]), s[11]); b = __builtin_fmaxf(__builtin_fmaxf(b, s[12]), s[13]);
  a = __builtin_fmaxf(__builtin_fmaxf(a, s[14]), s[15]);
  return __builtin_fmaxf(a, b);
}
DEVI float max2(float a) {
  auto t = __builtin_amdgcn_permlane32_swap(__float_as_uint(a), __float_as_uint(a), false, false);
  return __builtin_fmaxf(__uint_as_float(t[0]), __uint_as_float(t[1]));
}
struct K2S {
  f32x16 s[2][2], o[2], acc[3];
  u32x4 p[2][2];
  bf16x8 qm[2], km, v0, v1, f[2][4], wf[30];
  s16x4 qr[2], kr;
  lds_ptr kmp, krp, vp[4];
  const char *hp, *wp, *src, *wo, *we, *fp;
  unsigned dst, l16, woff, wdst, fo, fg, f3, f0, fz, ss, s5;
  rsrc_t rs;
  unsigned long long msk;
};
enum { Q8A, Q8B, P1A, P1B, Q16A, Q16B, P0A, P0B, G0, G1, G2 };
CE int k2_slot(int ng, int op) {
  constexpr int pos0[8] = {0, 6, 12, 18, 24, 30, 36, 42}, id0[8] = {Q8A, Q8B, P1A, P1B, Q16A, Q16B, P0A, P0B};
  constexpr int pos2[10] = {0, 4, 9, 14, 19, 24, 28, 33, 38, 43}, id2[10] = {Q8A, Q8B, G0, P1A, P1B, Q16A, Q16B, G1, P0A, P0B};
  constexpr int pos3[11] = {0, 4, 9, 13, 17, 22, 26, 30, 35, 39, 43}, id3[11] = {Q8A, Q8B, G0, P1A, P1B, G1, Q16A, Q16B, G2, P0A, P0B};
  for (int i = 0; i < 8 + ng; ++i) if ((ng == 0 ? pos0[i] : ng == 2 ? pos2[i] : pos3[i]) == op) return ng == 0 ? id0[i] : ng == 2 ? id2[i] : id3[i];
  return -1;
}
template <int J> DEVI void k2_fill(K2S& z) {
  if constexpr (J < 5) { glds<J * 8192, true>(z.src, z.l16, z.dst); z.src += z.ss; }
  else { glds_m(z.src - z.woff, z.l16, z.dst - z.woff + 40960, z.msk); z.src += z.s5; }
}
template <int NU, int S, int u> DEVI void k2_ldw(K2S& z) {
  asm volatile("global_load_dwordx4 %0, %1, %2 offset:%3 sc1" : "=v"(z.f[S][u]) : "v"(z.l16), "s"(z.wp), "n"(u * 1024) : "memory");
  if constexpr (u == NU - 1) { asm volatile("global_load_dwordx4 %0, %1, %2 sc1" : "=v"(z.f[S][3]) : "v"(z.l16), "s"(z.hp) : "memory"); z.hp += 1024; z.wp += 5120; }
}
CE int wo_iu(int NU, int F) { return NU == 3 ? (F < 9 ? F / 3 * 5 + F % 3 : F < 14 ? (F - 4) * 5 : F < 20 ? (F - 5) / 3 * 5 + (F - 5) % 3 : (F / 2 - 5) * 5 + 1 + F % 2) : F < 9 ? F * 5 : F < 19 ? (F - 9) * 5 + 1 : 45; }
CE int wo_id(int NU, int i, int u) { for (int F = 0; F < 10 * NU; ++F) if (wo_iu(NU, F) == 5 * i + u) return F; return -1; }
template <int NU, int F> DEVI void k2_ldo(K2S& z) {
  if constexpr (NU == 3 && F == 14) glds<9 * 1024, true>(z.wo + wo_iu(NU, F) * 1024, z.l16, z.wdst);
  else asm volatile("global_load_dwordx4 %0, %1, %2 sc1" : "=v"(z.wf[F]) : "v"(z.l16), "s"(z.wo + wo_iu(NU, F) * 1024) : "memory");
  SBAR();
}
template <int NU, int A, int B> DEVI void k2_ldos(K2S& z) { if constexpr (A < B) { k2_ldo<NU, A>(z); k2_ldos<NU, A + 1, B>(z); } }
constexpr int NL1 = 5, NL2 = 10, GP = 1, GS = 0, GBR = 4, F2T = 0, F2O = 1, FLT = 4, FLO = 24, FIT = 0, H1T = 0, H1O = 45;
CE int wo_at(int NU, int M, int I, int OP) {
  if (M == 0 && I == 4 && OP % 6 == 5 && OP / 6 < (NU == 3 ? 1 : 6)) return 14 + OP / 6;
  return M == 2 && NU == 3 && OP % 8 == 3 && OP / 8 < NL1 ? 15 + OP / 8 : -1;
}
CE int vm_ops(int NU, int M, int I, int a, int b) {
  int n = 0;
  for (int op = a; op < b; ++op) {
    const int m = k2_slot(NU, op);
    if (M == 0) n += (I == F2T && op == F2O) + (I == FIT && op % 8 == 3) + (I == 0 && op == 2) + (I == 5 && m - G0 == NU - 1) + 2 * (I == H1T && op == H1O) + (I == FLT && op == FLO) + (I > 3 && op == 41);
    if (M == 1) n += (op == 41 && I < 6) + (op == 20 && (I & 1) && I < 6) + (op == 3 && I == 0);
    n += wo_at(NU, M, I, op) >= 0;
    if ((M || I > 4) && m >= G0) n += m - G0 == NU - 1 ? 2 : 1;
  }
  return n;
}
CE int vm_peel(int NU, int I, int a, int J, int b) { int n = -vm_ops(NU, 0, I, 0, a) + vm_ops(NU, 0, J, 0, b); for (; I < J; ++I) n += vm_ops(NU, 0, I, 0, 48); return n; }
CE int vm_wait(int NU, int M, int I) { return vm_ops(NU, 1, (I + 6) & 7, 36, 48) + vm_ops(NU, 1, (I + 7) & 7, 0, 48) + vm_ops(NU, M, I, 0, 9); }
static_assert(vm_peel(3, 5, 36, 7, 0) >= vm_wait(3, 1, 3) && vm_peel(2, 5, 36, 7, 0) >= vm_wait(2, 1, 3) && vm_ops(2, 0, 6, 36, 48) >= vm_ops(2, 1, 2, 36, 48) && F2T * 48 + F2O < H1T * 48 + H1O && FIT * 48 + 43 < H1T * 48 + H1O && H1T * 48 + H1O < FLT * 48 + FLO && FLT * 48 + FLO < 5 * 48 + 33, "first tiles");
template <int NU, int P, int VM> DEVI void k2_got(K2S& z) {
  if constexpr (NU == 3) asm volatile("s_waitcnt vmcnt(%4)" : "+v"(z.f[P][0]), "+v"(z.f[P][1]), "+v"(z.f[P][2]), "+v"(z.f[P][3]) : "n"(VM) : "memory");
  else asm volatile("s_waitcnt vmcnt(%3)" : "+v"(z.f[P][0]), "+v"(z.f[P][1]), "+v"(z.f[P][3]) : "n"(VM) : "memory");
}
template <int NU, int M, int I, int OP = 0> DEVI void k2_tile(K2S& z, FS& f) {
  constexpr bool QK = M < 2;
  constexpr int P = I & 1, m = k2_slot(M ? NU : 0, OP), off = (M ? I : I & 3) * KVT, g = k2_slot(NU, OP) - G0;
  if constexpr (wo_at(NU, M, I, OP) >= 0) k2_ldo<NU, wo_at(NU, M, I, OP)>(z);
  if constexpr (M == 0 ? I == FLT && OP == FLO : M == 1 && I == 0 && OP == 3)
    asm volatile("global_load_dword %0, %1, %2 sc1" : "=v"(z.fg) : "v"(z.fo), "s"(z.fp) : "memory");
  if constexpr (M == 0 && I == F2T && OP == F2O) { asm volatile("s_waitcnt vmcnt(%0)" :: "n"(vm_peel(NU, 0, 0, I, OP)) : "memory"); st4(z.rs, z.f3, 0, f.tag); }
  if constexpr (M == 0 && I == 0 && OP == 2) asm volatile("global_load_dword %0, %1, %2 sc1" : "=v"(z.f0) : "v"(z.fz), "s"(f.ws) : "memory");
  if constexpr (M == 0 && I == 3 && OP == 46) asm volatile("v_writelane_b32 %0, %1, 1" : "+v"(z.f3) : "s"(f.fm));
  if constexpr (M == 0 && I == 4 && OP == 1) spin1(f.ws, z.fz, z.f0, f.tag);
  if constexpr (M == 0 && I == H1T && OP == H1O) { st16(z.rs, f.hl, f.hd[0], f.hb[0]); st16(z.rs, f.hl, f.hd[1], f.hb[1]); }
  if constexpr (M == 0 && I == FIT && OP % 8 == 3) k2_fill<OP / 8>(z);
  if constexpr (M == 0 && I == 5 && g == NU - 1) {
    asm volatile("s_waitcnt vmcnt(%1)" : "+v"(z.fg) : "n"(vm_peel(NU, FLT, FLO + 1, I, OP)) : "memory");
    st4(z.rs, z.f3, 128, f.tag);
    spin1(z.fp, z.fo, z.fg, f.tag);
  }
  if constexpr (M == 0 && I > 4 && g >= 0) k2_ldw<NU, P, g>(z);
  if constexpr (M == 0 && I == 6 && OP == 2) {
    const unsigned d = 8 * ((f.w >> 1) & 1);
    z.fp += 128 - d; z.fo = z.l16 < 512 ? z.fo : (z.l16 >> 2) + d; PIN(z.fo);
  }
  if constexpr (M < 2 && OP == 41 && (M ? I < 6 : I > 3)) k2_fill<M ? I : I & 3>(z);
  if constexpr (M == 1 && OP == 20 && (I & 1) && I < 6) glds<(I / 2) * 1024, true>(z.we + wo_iu(NU, I / 2) * 1024, z.l16, z.wdst);
  f32x16 (&S)[2] = z.s[P], (&N)[2] = z.s[P ^ 1];
  if constexpr (QK && m == Q8A) N[0] = MFMA8(z.kr, z.qr[0], f32x16{});
  if constexpr (QK && m == Q8B) { N[1] = MFMA8(z.kr, z.qr[1], f32x16{}); SBAR(); z.kr = ldl<s16x4>(z.krp + off); }
  if constexpr (m == P1A) z.o[0] = MFMA16(z.v1, BC(z.p[1][0]), z.o[0]);
  if constexpr (m == P1B) { z.o[1] = MFMA16(z.v1, BC(z.p[1][1]), z.o[1]); SBAR(); z.v1 = ldtr2(z.vp[2] + off, z.vp[3] + off); }
  if constexpr (QK && m == Q16A) N[0] = MFMA16(z.km, z.qm[0], N[0]);
  if constexpr (QK && m == Q16B) { N[1] = MFMA16(z.km, z.qm[1], N[1]); SBAR(); z.km = ldl<bf16x8>(z.kmp + off); }
  if constexpr (m == P0A) z.o[0] = MFMA16(z.v0, BC(z.p[0][0]), z.o[0]);
  if constexpr (m == P0B) { z.o[1] = MFMA16(z.v0, BC(z.p[0][1]), z.o[1]); SBAR(); z.v0 = ldtr2(z.vp[0] + off, z.vp[1] + off); }
  if constexpr (m == G0) {
    k2_got<NU, P, vm_wait(NU, M, I)>(z);
    if constexpr (M == 1 && I == 2) spin1(z.fp, z.fo, z.fg, f.tag);
  }
  if constexpr (m >= G0) {
    z.acc[g] = MFMA16(z.f[P][g], z.f[P][3], z.acc[g]); SBAR();
    k2_ldw<NU, P, g>(z);
  }
  SBAR();
  constexpr int pr = OP / 3, k = OP % 3;
  if constexpr (k < 2) { constexpr int e = (pr >> 3) * 8 + (pr & 3) * 2 + k; S[(pr >> 2) & 1][e] = __builtin_amdgcn_exp2f(S[(pr >> 2) & 1][e]); }
  else if constexpr (pr == 0) z.p[1][1][3] = cvtpk(N[1][14], N[1][15]);
  else { constexpr int q = pr - 1, e = (q >> 3) * 8 + (q & 3) * 2; z.p[q >> 3][(q >> 2) & 1][q & 3] = cvtpk(S[(q >> 2) & 1][e], S[(q >> 2) & 1][e + 1]); }
  SBAR();
  if constexpr (OP < 47) k2_tile<NU, M, I, OP + 1>(z, f);
}
#define APRIO(x) __builtin_amdgcn_s_setprio(x)
DEVI void k2_move(K2S& z, int d) { z.kmp += d; z.krp += d; z.vp[0] += d; z.vp[1] += d; z.vp[2] += d; z.vp[3] += d; }

constexpr int GA = 16, GC = 36, GD = 46;
template <int kh> DEVI void attend(const char* __restrict__ x, const char* __restrict__ b2, char* __restrict__ ws, char* __restrict__ out, FS f, u32x4 sw) {
  constexpr int NU = 3 - kh, L = NU + 1, PB = (1 - kh) * 6 * KVT;
  unsigned t = threadIdx.x;
  if constexpr (!kh) { PIN(t); asm volatile("" : "+s"(sw), "+s"(f.kv), "+s"(f.hp), "+s"(f.wp), "+s"(f.wo), "+s"(f.fp)); }
  const int w = f.w = sw[0], r = t & 31, hh = t >> 5 & 1;
  const unsigned l16 = f.l16;
  const int kq = w & 3, gmt = sw[1], nb = sw[2];
  const char* kv = f.kv;
  const rsrc_t rs = mkrs(ws);
  K2S z;
  z.kmp = (lds_ptr)(PB + l16); z.krp = (lds_ptr)(PB + f.kr);
#pragma unroll
  for (int i = 0; i < 4; ++i) z.vp[i] = (lds_ptr)(PB + f.vp[i]);
  const s16x4 krf = ldl<s16x4>(z.krp + kh * 6 * KVT);
  const bf16x8 kmf = ldl<bf16x8>(z.kmp + kh * 6 * KVT);
  SBAR();
  const s16x4 kr0 = ldl<s16x4>(z.krp);
  const bf16x8 km0 = ldl<bf16x8>(z.kmp);
  z.km = ldl<bf16x8>(z.kmp + KVT); z.kr = ldl<s16x4>(z.krp + KVT); z.v0 = ldtr2(z.vp[0] + KVT, z.vp[1] + KVT);
  SBAR();
  z.qm[0] = f.qm[0]; z.qm[1] = f.qm[1];
  f32x2 gt;
  g_fin<0, GA>(f, gt);
  {
    f32x16 sA = MFMA8(krf, f.qr[0], f32x16{}); SBAR();
    f32x16 sB = MFMA8(krf, f.qr[1], f32x16{}); SBAR();
    g_fin<GA, GA + 4>(f, gt);
    sA = MFMA16(kmf, z.qm[0], sA); SBAR();
    g_fin<GA + 4, GA + 8>(f, gt);
    sB = MFMA16(kmf, z.qm[1], sB); SBAR();
    g_fin<GA + 8, GA + 12>(f, gt);
    z.s[0][0] = MFMA16(km0, z.qm[0], f32x16{}); SBAR();
    g_fin<GA + 12, GC>(f, gt);
    float mA, mB;
    SET(mA, max16(sA)); SBAR();
    z.s[0][1] = MFMA16(km0, z.qm[1], f32x16{}); SBAR();
    SET(mB, max16(sB)); SBAR();
    u32x2 a = __builtin_bit_cast(u32x2, f.qr[0]), b = __builtin_bit_cast(u32x2, f.qr[1]);
    g_fin<GC, GC + 2>(f, gt);
    auto m = __builtin_amdgcn_permlane32_swap(__float_as_uint(mA), __float_as_uint(mB), false, false);
    unsigned c;
    SETU(c, cvtpk(-__builtin_fmaxf(__uint_as_float(m[0]), __uint_as_float(m[1])), 0.f));
    SETU(b[0], LM(0xffffffffull) ? b[0] : c); SBAR();
    g_fin<GC + 2, GC + 3>(f, gt);
    SETU(a[0], __builtin_amdgcn_permlane32_swap(a[0], c, false, false)[0]); SBAR();
    g_fin<GC + 3, GC + 5>(f, gt);
    z.qr[0] = __builtin_bit_cast(s16x4, a); z.qr[1] = __builtin_bit_cast(s16x4, b);
  }
  z.s[0][0] = MFMA8(kr0, z.qr[0], z.s[0][0]); SBAR();
  g_fin<GC + 5, GD>(f, gt);
  z.s[0][1] = MFMA8(kr0, z.qr[1], z.s[0][1]); SBAR();
  g_fin<GD, 60>(f, gt);
  z.s[1][0] = z.s[1][1] = z.o[0] = z.o[1] = f32x16{};
  z.v1 = bf16x8{};
  z.p[1][0] = z.p[1][1] = u32x4{};
  z.l16 = l16; z.woff = w * 1024; z.src = kv + z.woff; z.dst = CH + z.woff; z.msk = 0xffull << (8 * w); z.ss = 8192; z.s5 = 1024;
  z.hp = f.hp; z.wp = f.wp; z.we = z.wo = f.wo;
  const unsigned wreg = kh ? WE1 + kq * 9216 : WE0 + kq * 10240;
  z.wdst = wreg;
  z.fp = f.fp; z.fo = f.fo; z.f3 = f.f2; z.rs = rs; z.fz = f.fz;
  k2_move(z, 2 * KVT);
  SBAR();
  APRIO(3); k2_tile<NU, 0, 0>(z, f); APRIO(2); k2_tile<NU, 0, 1>(z, f); APRIO(1); k2_tile<NU, 0, 2>(z, f); APRIO(0); k2_tile<NU, 0, 3>(z, f);
  k2_move(z, CH + kh * 8 * KVT - PB - 2 * KVT);
  z.dst -= CH;
  asm volatile("s_waitcnt vmcnt(%1) lgkmcnt(0)\n\ts_barrier" : "+v"(z.f0) : "n"(vm_peel(NU, FIT, 44, 4, 0)) : "memory");
  APRIO(3);
 k2_tile<NU, 0, 4>(z, f); k2_tile<NU, 0, 5>(z, f); k2_tile<NU, 0, 6>(z, f);
  z.acc[0] = z.acc[1] = z.acc[2] = f32x16{};
  for (int c = 0; c < 3; ++c) {
    APRIO(2);
    k2_tile<NU, 1, 3>(z, f); k2_tile<NU, 1, 4>(z, f); k2_tile<NU, 1, 5>(z, f);
    APRIO(1);
    k2_tile<NU, 1, 6>(z, f);
    APRIO(0);
    k2_tile<NU, 1, 7>(z, f);
    k2_move(z, c == 1 ? CH : c == 0 ? -CH : kh ? -CH - 8 * KVT : 4 * KVT - CH);
    z.dst += c == 1 ? -CH : CH - 512 * c;
    WAIT_BAR(2 * L);
    APRIO(3);
    k2_tile<NU, 1, 0>(z, f); k2_tile<NU, 1, 1>(z, f); k2_tile<NU, 1, 2>(z, f);
    z.we += wo_iu(NU, 3) * 1024; z.wdst += 3072;
    if (c == 1) { z.src = z.wo + wo_iu(NU, 9) * 1024; z.ss = 5120; z.s5 = 0; }
  }
  APRIO(0); k2_tile<NU, 2, 3>(z, f);
  z.p[1][1][3] = cvtpk(z.s[1][1][14], z.s[1][1][15]);
  z.o[0] = MFMA16(z.v1, BC(z.p[1][0]), z.o[0]); z.o[1] = MFMA16(z.v1, BC(z.p[1][1]), z.o[1]);
  const unsigned gr = (t & 8) * 2 + ((t & 255) >> 4), gq = (t & 8) * 2 + (t & 16) / 2 + ((t & 255) >> 5);
  const unsigned go = (32 * gmt + gr) * 1280u + (t & 7) * 16, g2 = (32 * gmt + gq) * 1280u + 128 + (t & 7) * 8;
  const u32x4 xv = ldg<u32x4>(x + 320 * nb + 192 * kh, go), bv = ldg<u32x4>(b2 + 320 * nb + 192 * kh, (t & 7) * 16);
  u32x2 xw, bw;
  if (kh) { xw = ldg<u32x2>(x + 320 * nb, g2); bw = ldg<u32x2>(b2 + 320 * nb + 128, (t & 7) * 8); }
  SBAR();
  if constexpr (!kh) k2_ldos<NU, 15 + NL1, 15 + NL1 + NL2>(z);
  f32x16 o = kh ? z.o[1] : z.o[0];
  {
    const f32x16 g = kh ? z.o[0] : z.o[1];
    const lds_ptr xw = (lds_ptr)(w * 3072 + l16), xr = (lds_ptr)((w ^ 4) * 3072 + l16);
    LGKM_BAR();
#pragma unroll
    for (int i = 0; i < 3; ++i) stl<f32x4>(xw + i * 1024, f32x4{g[4 * i], g[4 * i + 1], g[4 * i + 2], g[4 * i + 3]});
    LGKM_BAR();
#pragma unroll
    for (int i = 0; i < 3; ++i) { const f32x4 q = ldl<f32x4>(xr + i * 1024); o[4 * i] += q[0]; o[4 * i + 1] += q[1]; o[4 * i + 2] += q[2]; o[4 * i + 3] += q[3]; }
  }
  auto lt = __builtin_amdgcn_permlane32_swap(__float_as_uint(o[8]), __float_as_uint(o[8]), false, false);
  float l = __uint_as_float(lt[1]);
  float om = 0.f;
#pragma unroll
  for (int i = 0; i < 12; ++i) om = __builtin_fmaxf(om, __builtin_fabsf(o[i]));
  const bool bad = !(l >= 0x1p-30f && l <= 0x1p60f && om <= 0x1p126f);
  if (__builtin_expect(__builtin_amdgcn_ballot_w64(bad) != 0, 0)) {
    asm volatile("buffer_inv sc1\n\ts_waitcnt vmcnt(0)" ::: "memory");
    const bf16x8 qm = kh ? z.qm[1] : z.qm[0];
    s16x4 qr = kh ? z.qr[1] : z.qr[0];
    if (hh) qr = s16x4{};
    const int vs = r < 16 ? 16 : r < 20 ? 8 : 0, vo = QT + (r < 16 ? (r >> 3) * 128 + (r & 7) * 2 : r < 20 ? 1024 + (r - 16) * 2 : r == 20 ? QT : QT + 8) + 4 * hh * vs, bs = vs * vs;
    float m = -__builtin_inff();
    o = f32x16{};
#pragma unroll 1
    for (int k = 0; k < 64; ++k) {
      const char *tp = kv + recpos(k) * KVT, *pa = kv + recpos(k + 1) * KVT, *pb = kv + recpos(k + 2) * KVT;
      const bf16x8 a = ldg<bf16x8>(tp, l16);
      const s16x4 b = ldg<s16x4>(tp, f.r8);
      bf16x8 va, vb;
#pragma unroll
      for (int j = 0; j < 8; ++j) { va[j] = ldg<short>(pa, vo + (j >> 2) * bs + (j & 3) * vs); vb[j] = ldg<short>(pb, vo + (2 + (j >> 2)) * bs + (j & 3) * vs); }
      f32x16 s = MFMA8(b, qr, f32x16{}); s = MFMA16(a, qm, s);
      const float mn = __builtin_fmaxf(m, max2(max16(s))), al = __builtin_amdgcn_exp2f(m - mn);
      m = mn;
      float p[16];
#pragma unroll
      for (int i = 0; i < 16; ++i) { p[i] = __builtin_amdgcn_exp2f(s[i] - mn); o[i] *= al; }
      o = MFMA16(va, BC(pack8(p)), o);
      o = MFMA16(vb, BC(pack8(p + 8)), o);
    }
    lt = __builtin_amdgcn_permlane32_swap(__float_as_uint(o[8]), __float_as_uint(o[8]), false, false);
    l = __uint_as_float(lt[1]);
  }
  const float il = __builtin_amdgcn_rcpf(l);
  {
    unsigned d[6];
#pragma unroll
    for (int i = 0; i < 6; ++i) d[i] = __builtin_elementwise_min(cvtpk(o[2 * i] * il, o[2 * i + 1] * il), 0xfffffffeu);
    o_st(f, rs, u32x4{d[0], d[1], d[2], d[3]}, u32x2{d[4], d[5]});
  }
  SBAR();
  if constexpr (!kh) k2_ldos<NU, 15 + NL1 + NL2, 30>(z);
  k2_got<NU, 0, 2 + (kh ? 0 : 15 - NL1 - NL2)>(z); k2_got<NU, 1, 63>(z);
#pragma unroll
  for (int i = 0; i < 2 * NU; ++i) z.acc[i % NU] = MFMA16(z.f[i / NU][i % NU], z.f[i / NU][3], z.acc[i % NU]);
  SBAR();
  unsigned wl = l16 + wreg, ga = l16 + z.woff + WL0, g8 = f.r8 + (l16 & 512) + z.woff + (WL0 + 31 * 1024);
  PIN(wl); PIN(ga);
#pragma unroll
  for (int j = 0; j < 15 - kh; ++j) { const int i = j < 5 ? j + 9 : j < 14 ? j - 5 : j; z.wf[i] = ldl<bf16x8>(j < 5 ? (lds_ptr)ga + j * 8192 : (lds_ptr)wl + (j - 5) * 1024); SBAR(); }
  const char* ob = ws + WS_OI + 2048 + (gmt * 8 + 2 * (kq ^ KQX * kh) + kh) * 5120;
#define K2_D(c, off) "s_add_u32 m0, %2, " #c "\n\ts_nop 0\n\tglobal_load_lds_dwordx4 %0, %1 offset:" #off " sc1\n\t"
#define K2_G(a, e) asm volatile(a K2_D(26624, -2048) K2_D(33792, -1024) K2_D(40960, 0) K2_D(48128, 1024) K2_D(55296, 2048) e :: "v"(l16), "s"(ob), "s"(z.woff), "n"(10 - kh) : "m0", "scc", "memory")
  if constexpr (GP == 0) K2_G("s_waitcnt lgkmcnt(%3)\n\t", "");
  f32x4 q[3];
#pragma unroll
  for (int u = 0; u < 2 + kh; ++u) {
#pragma unroll
    for (int i = 0; i < 2; ++i) {
      const unsigned a = u < 2 ? xv[2 * u + i] : xw[i], b = u < 2 ? bv[2 * u + i] : bw[i];
      q[u][2 * i] = blo(a) + blo(b); q[u][2 * i + 1] = bhi(a) + bhi(b);
    }
    PIN(q[u]);
  }
  unsigned rw = wreg + r * 272 + hh * 16, w2 = RD2 + kq * 4608 + r * 144 + hh * 16, rr = (kh ? WE1 : WE0) + gr * 272 + (t & 7) * 32, r2 = RD2 + gq * 144 + (t & 7) * 16, o2 = t & 1 ? DROP : g2;
  PIN(rw); PIN(rr);
  if (kh) { PIN(r2); PIN(o2); } else PIN(w2);
  unsigned g9 = g8 + 256;
  PIN(g8); PIN(g9);
#pragma unroll
  for (int u = 0; u < NU; ++u) PIN(z.acc[u]);
  if constexpr (GP == 1) K2_G("s_waitcnt lgkmcnt(0)\n\t", "");
  u32x4 of[10];
  auto rd = [&](int b, unsigned x) {
#pragma unroll
    for (int i = 0; i < 4; ++i) of[b + i] = ldl<u32x4>((lds_ptr)(ga ^ x) + i * 8192);
    of[b + 4] = __builtin_shufflevector(ldl<u32x2>((lds_ptr)(g8 ^ x)), ldl<u32x2>((lds_ptr)(g9 ^ x)), 0, 1, 2, 3);
    SBAR();
  };
  asm volatile("s_waitcnt vmcnt(0)" ::: "memory");
#pragma unroll 1
  for (int spin = 0; spin < SPINS; ++spin) {
    rd(5 * kh, 0);
    unsigned all = 0;
#pragma unroll
    for (int i = 0; i < 20; ++i) all = __builtin_elementwise_max(all, of[5 * kh + (i >> 2)][i & 3]);
    if (__builtin_amdgcn_ballot_w64(all == ~0u) == 0) break;
    if constexpr (GS > 0) __builtin_amdgcn_s_sleep(GS);
    K2_G("", "s_waitcnt vmcnt(0)");
  }
#pragma unroll
  for (int i = 15 - kh; i < 10 * NU; ++i) PIN(z.wf[i]);
  SBAR();
#pragma unroll
  for (int n = 0; n < 10 * NU + 4; ++n) {
    const int m = n - 5 * NU, y = 5 * NU - 5, u = m < 0 ? n % NU : m < y ? m % (NU - 1) : NU - 1, i = m < 0 ? 5 * kh + n / NU : 5 - 5 * kh + (m < y ? m / (NU - 1) : m - y), e = m - y - 1;
    if (n == GBR * NU) { asm volatile("s_barrier" ::: "memory"); rd(5 - 5 * kh, 6144); }
    if (n < 10 * NU) { z.acc[u] = MFMA16(z.wf[wo_id(NU, i, u)], BC(of[i]), z.acc[u]); PIN(z.acc[u]); SBAR(); }
#pragma unroll
    for (int k = e < 4 ? e * (NU - 1) : e + 4 * NU - 8; e >= 0 && k < (e < 4 ? (e + 1) * (NU - 1) : e + 4 * NU - 7); ++k) {
      const int v = k >> 2, d = k & 3;
      stl<f32x4>((v < 2 ? (lds_ptr)rw + v * 128 : (lds_ptr)w2) + d * 32, f32x4{z.acc[v][4 * d], z.acc[v][4 * d + 1], z.acc[v][4 * d + 2], z.acc[v][4 * d + 3]}); SBAR();
    }
  }
  LGKM_BAR();
  if (kh == 0) asm volatile("fused_code_b:");
  const rsrc_t ro = mkrs(out);
  unsigned d[6];
  f32x4 pa[3][4];
#pragma unroll
  for (int i = 0; i < 8 + 4 * kh; ++i) { pa[i >> 2][i & 3] = ldl<f32x4>(i < 8 ? (lds_ptr)rr + 16 * (i >> 2) + (i & 3) * (kh ? 9216 : 10240) : (lds_ptr)r2 + (i & 3) * 4608); SBAR(); }
#pragma unroll
  for (int u = 0; u < 2 + kh; ++u) {
#pragma unroll
    for (int s = 0; s < 4; ++s) q[u] += pa[u][s];
    d[2 * u] = cvtpk(q[u][0], q[u][1]); d[2 * u + 1] = cvtpk(q[u][2], q[u][3]);
    if (u == 1) st16(ro, go + 192 * kh, 320 * nb, u32x4{d[0], d[1], d[2], d[3]});
  }
  if (kh) st16(ro, o2, 320 * nb, u32x4{d[4], d[5], (unsigned)__builtin_amdgcn_mov_dpp(d[4], 0xb1, 0xf, 0xf, false), (unsigned)__builtin_amdgcn_mov_dpp(d[5], 0xb1, 0xf, 0xf, false)});
  if (!kh && blockIdx.x == 0) st4(rs, t < 16 ? t * 4 : DROP, 0, f.tag);
}

constexpr int HSLEEP = 8, PTCH = 1, HPRE = 2;
struct RL { const char* src; unsigned vo, rs, dst; };
DEVI RL rl_adr(const char* w2, const char* wo, int ks, int lane) {
  const int hw = ks - 108, r0 = hw < 0 ? 16 * ks : hw < 32 ? 20 * hw + 4 * (hw & 1) : 80 * (hw - 32) + 16, r1 = (unsigned)hw < 32u ? 8 : 4;
  return RL{(hw < 0 ? w2 : wo) + r0 * 1280, ((lane >> 5) * r1 + ((lane >> 3) & 3)) * 1280u + (lane & 7) * 16, (hw < 0 ? 8 : hw < 32 ? 4 : 40) * 1280u, WS_W2I + (hw < 0 ? hpos(ks) : hw < 32 ? ks + (hw >> 2) : 5 * hw - 48) * 5120};
}
DEVI void f_drop(FS& f) {
  asm volatile("" : "=v"(f.hb[0]), "=v"(f.k1), "=v"(f.k2), "=v"(f.sd), "=v"(f.ga[0]), "=v"(f.ga[1]), "=v"(f.ga[2]), "=v"(f.ga[3]), "=v"(f.ga[4]), "=v"(f.ga[5]), "=v"(f.ga[6]), "=v"(f.ga[7]));
}
CE unsigned rl_off(int u) { return (u / 5) * 148 * 5120 + (u % 5) * 1024; }
__global__ __launch_bounds__(512, 2) void fused(const char* x, const char* wqkv, const char* wo, const char* w1, const char* b1, const char* w2, const char* b2, char* ws, char* out) {
  unsigned t0;
  {
    const char* pc; unsigned u;
    asm volatile("s_cmp_lt_u32 %3, 240\n\ts_cbranch_scc1 1f\n\ts_getpc_b64 %1\nfused_code_a:\n\ts_bfe_u32 %2, %3, 0x10003\n\ts_lshl_b32 %2, %2, 15\n\tv_lshl_add_u32 %0, %4, 6, %2\n\t"
                 "v_min_u32 %0, fused_code_b-fused_code_a, %0\n\tglobal_load_dword %0, %0, %1\n1:" : "=&v"(t0), "=&s"(pc), "=&s"(u) : "s"(blockIdx.x), "v"(threadIdx.x) : "scc", "memory");
  }
  asm volatile("" : "+s"(x), "+s"(wqkv), "+s"(wo), "+s"(w1), "+s"(b1), "+s"(w2), "+s"(b2), "+s"(ws), "+s"(out));
  const int tid = threadIdx.x, lane = tid & 63, w = __builtin_amdgcn_readfirstlane(tid >> 6), bid = blockIdx.x, x8 = bid & 7, j = bid >> 3;
  const int rt = w & 3, ch = w >> 2, row4 = (lane & 15) >> 2;
  const rsrc_t rs = mkrs(ws);
  FS f;
  u32x8 sv;
  u32x4 sw;
  f.l16 = lane * 16; f.ws = ws; f.w = w;
  if (j >= 30) {
    const int hg = 2 * x8 + j - 30, id = 8 * hg + w, e = (id * 205) >> 10, v = id - 5 * e;
    unsigned tg;
    ST st;
    unsigned pt = id * 704 + lane * 64;
    if constexpr (PTCH) asm volatile("s_mov_b64 exec, 0x7ff\n\tglobal_load_dword %0, %0, %1 sc1\n\ts_mov_b64 exec, -1" : "+v"(pt) : "s"(ws) : "memory");
    st_ld(st, x + (128 * hg + 120 + w) * 1280, f.l16);
    asm volatile("global_load_dword %0, %1, %2 sc1" : "=v"(tg) : "v"(0), "s"(ws) : "memory");
    const RL a = rl_adr(w2, wo, id, lane), b = rl_adr(w2, wo, id < 100 ? 128 + e : 128, lane);
    const unsigned lb = w * 20480;
#pragma unroll
    for (int i = 0; i < HPRE; ++i) glds(a.src + (i >> 1) * 128 + (i & 1) * a.rs, a.vo, lb + i * 1024);
    asm volatile("s_waitcnt vmcnt(%5)" : "+v"(t0), "+v"(tg), "+v"(st.a), "+v"(st.b), "+v"(pt) : "n"(HPRE) : "memory");
    f.tag = tag_of(tg);
    st_all(st);
    st8(rs, (lane & 31) == 16 ? lane >> 2 & 8 : DROP, WS_ST + (128 * hg + 120 + w) * 16, u32x2{__float_as_uint(st.s[0]), f.tag});
#pragma unroll
    for (int i = HPRE; i < 20; ++i) glds(a.src + (i >> 1) * 128 + (i & 1) * a.rs, a.vo, lb + i * 1024);
    f.f2 = DROP;
    f_adr<0>(f); f_adr<1>(f); f_adr<2>(f); f_adr<3>(f); f_adr<4>(f); f_adr<5>(f); f_adr<6>(f); f_adr<7>(f); f_adr<8>(f); f_adr<9>(f);
    o_mark(f, rs);
    const lds_ptr rp = (lds_ptr)(lb + (8 * (lane >> 5) + row4) * 128 + 32 * ((lane >> 4) & 1) + 8 * (lane & 3));
#pragma unroll
    for (int i = 0; i < 20; ++i) {
      if (i < 4) asm volatile("s_waitcnt vmcnt(20)" ::: "memory"); else asm volatile("s_waitcnt vmcnt(24)" ::: "memory");
      st16(rs, f.l16, a.dst + rl_off(i), __builtin_bit_cast(u32x4, ldtr2(rp + (i >> 1) * 2048 + (i & 1) * 64, 512)));
      if (i == 3)
#pragma unroll
        for (int k = 0; k < 4; ++k) glds(b.src + (2 * v + (k >> 1)) * 128 + (k & 1) * b.rs, b.vo, lb + k * 1024);
    }
    asm volatile("s_waitcnt vmcnt(16)" ::: "memory");
#pragma unroll
    for (int i = 0; i < 4; ++i) st16(rs, id < 100 ? f.l16 : DROP, b.dst + rl_off(4 * v + i), __builtin_bit_cast(u32x4, ldtr2(rp + (i >> 1) * 2048 + (i & 1) * 64, 512)));
    f_wait<HSLEEP>(f);
    WAIT_BAR(0);
    f_all(f);
    st4(rs, tid ? DROP : WS_F0 + hg * 4, 0, f.tag);
    f_got<1>(f);
    f.hl = DROP;
    sv = u32x8{f.tag, 0, f.rd, f.om, f.orr, f.fm, 0, 0}; sw = u32x4{f.w, f.gmt, f.nb, f.h};
    SPIN(sv); SPIN(sw);
    f_drop(f);
  } else {
    const unsigned L = 30 * x8 + j, mhalf = L >= 120, n = (L - 120 * mhalf) >> 3, mg = 8 * mhalf + (L & 7), r = lane & 31, hh = lane >> 5, mt = 4 * mg + rt;
    P1 z;
    z.w = w; z.tid = tid; z.ldst = w * 1024; z.rs = rs;
    z.vx = (tid >> 3) * 1280 + ((tid & 7) ^ ((tid >> 4) & 7)) * 16;
    z.xb[0] = x + mg * 128 * 1280; z.xb[1] = z.xb[0] + 64 * 1280; z.qb = wqkv + 256 * n;
    p_lds<0, 2>(z);
    const unsigned wr = 8 * w + ((lane >> 2) & 7), wc = hh * 64 + (lane & 3) * 16;
    z.vq = wr * 3840 + wc;
    p_lds<2, 4>(z);
    u32x16 A, C;
    u32x8 B;
    asm volatile("s_load_dwordx16 %0, %3, %4\n\ts_load_dwordx16 %1, %3, %4 offset:64\n\ts_load_dwordx8 %2, %3, %5 offset:3840"
                 : "=&s"(A), "=&s"(C), "=&s"(B) : "s"(&TAB), "s"((2 * n + ch) * 128), "s"((((n * 13) >> 6) * 64 + mt) * 32));
    SBAR();
    st_ld(z.st, z.xb[0] + (8 * n + w) * 1280, f.l16);
    {
      const unsigned bi = 128 * n + 64 * hh + 32 * ch + (r & 19) + ((r & 4) << 1) + ((r & 8) >> 1);
      asm volatile("global_load_ushort %0, %2, %3\n\tglobal_load_dword %1, %4, %5 sc1" : "=&v"(z.bias), "=&v"(z.tg) : "v"((bi < FF ? bi : 0) * 2), "s"(b1), "v"(0), "s"(ws) : "memory");
      SBAR();
    }
    z.vm = wr * 3456 + wc;
    const unsigned tw = rt ^ ch, xa = (32 * rt + r) * 128 + (((2 * tw + hh) ^ ((r >> 1) & 7)) * 16);
    const unsigned wa = 5 * 16384 + ch * 8192 + (2 * tw + hh) * 1024 + row4 * 64 + (lane & 16) * 2 + (lane & 1) * 16 + (lane & 2) * 4;
#pragma unroll
    for (int p = 0; p < 4; ++p) { z.xa[p] = xa ^ (p * 32); z.wa[p] = wa ^ (p * 2048); PIN(z.xa[p]); PIN(z.wa[p]); }
    PIN(z.vm);
    SBAR();
    z.pq = wqkv + (256 * n + (QR0 + (640 - QR0) / 64 * (8 * (L & 7) + w)) * 3840); z.pw = w1 + ((n < 14 ? 256 * n : 0) + 10 * (8 * (L & 7) + w) * 3456); z.pe = n < 13 ? 0xfffff : 0x55555;
    z.pf[0] = (lane >> 1) * 3840 + (lane & 1) * 128; z.pf[1] = (lane >> 1) * 3456 + (lane & 1) * 128;
    p_lds<4, PQH>(z);
    if constexpr (PQG < 0) l2_q(z);
    p_lds<PQH, NP>(z);
    if constexpr (PMG < 0) l2_m(z);
    const short one = LM(0x1010010110100101ull) ? (short)0x3f80 : (short)0;
    z.sel = bf16x8{one, one, one, one, one, one, one, one};
    z.sa = (32 * rt + r) * 16 + hh * 8; z.sp = ws + (WS_ST + mg * 2048);
    z.ca = CS + ch * 1024 + r * 16; z.cw = z.ca + rt * 4; z.hm = hh ? ~0u : 0u;
    z.wx = -16384 - ch * 7680; z.odd = 0xAAAAAAAAAAAAAAAAull; z.low = 0xffffffffull; z.hi = 0xffff0000u; z.pm = 0x07060302u;
    unsigned kd = row4 * KVH + (lane & 3) * 16 + (lane < 16 ? 0 : DROP);
    PIN(kd); PIN(z.sel); PIN(z.sa); PIN(z.ca); PIN(z.cw); PIN(z.hm);
    SPIN(z.wx); SPIN(z.odd); SPIN(z.low); SPIN(z.hi); SPIN(z.pm); SPIN(z.sp);
    SBAR();
    asm volatile("s_waitcnt lgkmcnt(0)" : "+s"(A), "+s"(C), "+s"(B));
#pragma unroll
    for (int t = 0; t < 4; ++t) {
      z.rm[t] = A[2 * t] | (unsigned long long)A[2 * t + 1] << 32; z.qo[t][0] = A[8 + 2 * t]; z.qo[t][1] = A[9 + 2 * t]; z.hd[t >> 1][t & 1] = C[t];
    }
    const unsigned rb = r < 16 ? B[0] : B[1];
    z.lm = rb + __umul24(r >> 3, C[10]) + (r & 7) * 16;
    z.lr = rb + ((r & 30) * 8 + 1024) + ((r & 1) ? C[13] : 0);
    z.hl = f.l16 + (B[3] + C[8]);
    z.kd = kd + (B[2] + C[5]);
    z.sc = B[5]; z.n5 = C[6]; z.fs = B[6]; f.f2 = B[4] + C[4]; z.so = B[7] + C[7]; z.hx = C[9]; z.mb[0] = w1 + C[14]; z.mb[1] = w1 + C[15];
    SPIN(f.f2); SPIN(z.so); SPIN(z.mb[0]); SPIN(z.mb[1]);
    PIN(z.lm); PIN(z.lr); PIN(z.hl); PIN(z.kd);
    SBAR();
    WAIT_BAR(bar_vm(-2));
    a_ldw<0, 0>(z); p_ldx<0>(z); a_ldw<0, 1>(z); a_ldw<1, 0>(z); p_ldx<1>(z); a_ldw<1, 1>(z); p_ldx<2>(z);
    a_step<0>(z, f);
    b_step<40>(z, f);
    g_ops<1, 0, GO>(z);
    f.hl = z.hl + z.hx; PIN(f.hl);
    g_ops<1, GO, GO + 60>(z);
#pragma unroll
    for (int i = 0; i < 8; ++i) f.ga[i] = z.am[1][8 + i];
    f.k1 = z.k1; f.k2 = z.k2; f.sd = z.sd; f.hb[0] = z.pk[0];
    sv = u32x8{f.tag, f.no, f.rd, f.om, f.orr, f.fm, z.hd[1][0], z.hd[1][1]}; sw = u32x4{f.w, f.gmt, f.nb, f.h};
    SPIN(sv); SPIN(sw);
    f_got<vm_in(8 * (KFL + KFS + 7 * KLS) + 4, 999)>(f);
  }
  SPIN(sv);
  f.tag = sv[0]; f.no = sv[1]; f.rd = sv[2]; f.om = sv[3]; f.orr = sv[4]; f.fm = sv[5]; f.hd[0] = sv[6]; f.hd[1] = sv[7];
  if (f.no) { f_wait<BSLEEP>(f); f_all(f); f_got<0>(f); }
  LGKM_BAR();
  if (w >= 4) {
    attend<1>(x, b2, ws, out, f, sw);
    asm volatile("s_endpgm" : "=v"(f.qm[0]), "=v"(f.qm[1]), "=v"(f.qr[0]), "=v"(f.qr[1]), "=v"(f.l16), "=v"(f.r8), "=v"(f.hl), "=v"(f.kr), "=v"(f.fo), "=v"(f.f2), "=v"(f.fz), "=v"(f.vp[0]), "=v"(f.vp[1]), "=v"(f.vp[2]), "=v"(f.vp[3]) :: "memory");
    f_drop(f);
  }
  attend<0>(x, b2, ws, out, f, sw);
}

extern "C" void kernel_launch(void* const* d_in, const int* in_sizes, int n_in, void* d_out, int out_size, void* d_ws, size_t ws_size,
                              hipStream_t stream) {
  (void)hipFuncSetAttribute((const void*)fused, hipFuncAttributeMaxDynamicSharedMemorySize, LDS_BYTES);
  fused<<<256, 512, LDS_BYTES, stream>>>((const char*)d_in[0], (const char*)d_in[1], (const char*)d_in[2], (const char*)d_in[4], (const char*)d_in[5],
                                         (const char*)d_in[6], (const char*)d_in[7], (char*)d_ws, (char*)d_out);
}
```
